```python
import torch

def _softmax(x, axis=-1):
    return torch.softmax(x, dim=axis)
def _gelu(x):
    return torch.nn.functional.gelu(x, approximate="tanh")
def _silu(x):
    return torch.nn.functional.silu(x)
def _sigmoid(x):
    return torch.sigmoid(x)
def _layernorm(x, axis=-1, eps=1e-5):
    m = x.mean(dim=axis, keepdim=True)
    v = x.var(dim=axis, keepdim=True, unbiased=False)
    return (x - m) / torch.sqrt(v + eps)
def _scatter_add(tgt, idx, src, axis=0):
    return tgt.clone().index_add_(axis, idx, src)
def _segment_sum(src, seg, n):
    out = torch.zeros((n,) + src.shape[1:], dtype=src.dtype)
    return out.index_add_(0, seg, src)
def _pad(x, pw):
    flat = tuple(v for pair in reversed(pw) for v in pair)
    return torch.nn.functional.pad(x, flat)

kernel_name = 'concat_heads__grouped_kv_mqa__sliding_local_dilated__prompt'

def setup_inputs():

    _w = (lambda a: (lambda t: t.to(torch.bfloat16) if t.is_floating_point() else t)(torch.as_tensor(a)))
    def _A_setup():
        B, S, D, H = 1, 2048, 512, 32
        Dh = D // H
        return {
            "x": _w((lambda *s: torch.randn(*s, dtype=torch.bfloat16) * 0.05)(B, S, D)),
            "wq": _w((lambda *s: torch.randn(*s, dtype=torch.bfloat16) * 3.2)(D, H * Dh)),
            "wk": _w((lambda *s: torch.randn(*s, dtype=torch.bfloat16) * 3.2)(D, Dh)),
            "wv": _w((lambda *s: torch.randn(*s, dtype=torch.bfloat16) * 1.6)(D, Dh)),
            "wo": _w((lambda *s: torch.randn(*s, dtype=torch.bfloat16) * 1.6)(H * Dh, D)),
        }
    _A_in = _A_setup()

    def _B_setup():
        B, S, D = 1, 2048, 512
        return {
            "x": _w((lambda *s: torch.randn(*s, dtype=torch.bfloat16) * 0.1)(B, S, D)),
            "wqkv": _w((lambda *s: torch.randn(*s, dtype=torch.bfloat16) * 0.8)(D, 3 * D)),
            "wo": _w((lambda *s: torch.randn(*s, dtype=torch.bfloat16) * 0.8)(D, D)),
        }
    _B_in = _B_setup()

    return {
        "A_x": _A_in["x"],
        "A_wq": _A_in["wq"],
        "A_wk": _A_in["wk"],
        "A_wv": _A_in["wv"],
        "A_wo": _A_in["wo"],
        "B_x": _B_in["x"],
        "B_wqkv": _B_in["wqkv"],
        "B_wo": _B_in["wo"],
    }

def reference(A_x, A_wq, A_wk, A_wv, A_wo, B_x, B_wqkv, B_wo):
    def _A_ref(x, wq, wk, wv, wo):
        B, S, D = x.shape
        H = 32
        Dh = D // H
        q = (lambda x, *d: x.permute(*d))((x @ wq).reshape(B, S, H, Dh), 0, 2, 1, 3)
        k = x @ wk
        v = x @ wv
        scores = torch.einsum("bhqd,bkd->bhqk", q, k) * (Dh ** -0.5)
        p = _softmax(scores, axis=-1)
        o = torch.einsum("bhqk,bkd->bhqd", p, v)
        return (lambda x, *d: x.permute(*d))(o, 0, 2, 1, 3).reshape(B, S, H * Dh) @ wo

    def _B_ref(x, wqkv, wo):
        B, S, D = x.shape
        H = 32; Dh = D // H
        W, dil = 8, 2
        qkv = (lambda x, *d: x.permute(*d))((x @ wqkv).reshape(B, S, 3, H, Dh), 2, 0, 3, 1, 4)
        q, k, v = qkv[0], qkv[1], qkv[2]
        scale = Dh ** -0.5
        pos = torch.arange(S)
        off = torch.arange(W) * dil
        idx = pos[:, None] - off[None, :]
        mask = idx >= 0
        idx_c = torch.where(mask, idx, 0).reshape(-1)
        kg = k[:, :, idx_c, :].reshape(B, H, S, W, Dh)
        vg = v[:, :, idx_c, :].reshape(B, H, S, W, Dh)
        logits = torch.einsum("bhsd,bhswd->bhsw", q, kg) * scale
        logits = torch.where(mask[None, None, :, :], logits, -1e9)
        attn = _softmax(logits, axis=-1)
        o = torch.einsum("bhsw,bhswd->bhsd", attn, vg)
        return (lambda x, *d: x.permute(*d))(o, 0, 2, 1, 3).reshape(B, S, D) @ wo

    a = _A_ref(A_x, A_wq, A_wk, A_wv, A_wo)
    b = _B_ref(B_x, B_wqkv, B_wo)
    return torch.concatenate([a, b], axis=-1)
```

```cpp
#include <hip/hip_runtime.h>
#include <hip/hip_bf16.h>
typedef __hip_bfloat16 bf16;
__device__ inline float b2f(bf16 x){ return __bfloat162float(x);}
__device__ inline bf16 f2b(float x){ return __float2bfloat16(x);}

__global__ void gemm_naive(const bf16* A, const bf16* B, bf16* C, int M, int N, int K, int ldc){
    int n = blockIdx.x*64 + (threadIdx.x&63);
    int m = blockIdx.y*4 + (threadIdx.x>>6);
    if (n>=N||m>=M) return;
    float acc=0;
    for (int k=0;k<K;k++) acc += b2f(A[m*K+k])*b2f(B[k*N+n]);
    C[m*ldc+n]=f2b(acc);
}
__global__ void attnA_naive(const bf16* Q, const bf16* Kk, const bf16* V, bf16* O){
    __shared__ float ks[256][16]; __shared__ float vs[256][16];
    int idx = blockIdx.x*256+threadIdx.x;
    int pos = idx>>5, h = idx&31;
    float q[16]; for (int d=0;d<16;d++) q[d]=b2f(Q[pos*512+h*16+d])*0.25f;
    float m=-1e30f,l=0,o[16]; for(int d=0;d<16;d++) o[d]=0;
    for (int t=0;t<2048;t+=256){
        __syncthreads();
        for (int d=0;d<16;d++){ ks[threadIdx.x][d]=b2f(Kk[(t+threadIdx.x)*16+d]); vs[threadIdx.x][d]=b2f(V[(t+threadIdx.x)*16+d]); }
        __syncthreads();
        for (int j=0;j<256;j++){
            float s=0; for(int d=0;d<16;d++) s+=q[d]*ks[j][d];
            float mn=fmaxf(m,s); float a=__expf(m-mn); float p=__expf(s-mn);
            l=l*a+p; for(int d=0;d<16;d++) o[d]=o[d]*a+p*vs[j][d]; m=mn;
        }
    }
    for(int d=0;d<16;d++) O[pos*512+h*16+d]=f2b(o[d]/l);
}
__global__ void attnB_naive(const bf16* QKV, bf16* O){
    int idx = blockIdx.x*256+threadIdx.x; int pos=idx>>5,h=idx&31;
    float q[16]; for(int d=0;d<16;d++) q[d]=b2f(QKV[pos*1536+h*16+d]);
    float lg[8]; float m=-1e30f;
    for(int w=0;w<8;w++){ int j=pos-2*w; if(j>=0){ float s=0; for(int d=0;d<16;d++) s+=q[d]*b2f(QKV[j*1536+512+h*16+d]); lg[w]=s*0.25f;} else lg[w]=-1e9f; m=fmaxf(m,lg[w]); }
    float l=0; for(int w=0;w<8;w++){ lg[w]=__expf(lg[w]-m); l+=lg[w]; }
    float o[16]; for(int d=0;d<16;d++) o[d]=0;
    for(int w=0;w<8;w++){ int j=pos-2*w; if(j<0) j=0; for(int d=0;d<16;d++) o[d]+=lg[w]/l*b2f(QKV[j*1536+1024+h*16+d]); }
    for(int d=0;d<16;d++) O[pos*512+h*16+d]=f2b(o[d]);
}
extern "C" void kernel_launch(void* const* d_in, const int* in_sizes, int n_in, void* d_out, int out_size, void* d_ws, size_t ws_size, hipStream_t stream){
    const bf16* Ax=(const bf16*)d_in[0]; const bf16* wq=(const bf16*)d_in[1]; const bf16* wk=(const bf16*)d_in[2]; const bf16* wv=(const bf16*)d_in[3]; const bf16* Awo=(const bf16*)d_in[4];
    const bf16* Bx=(const bf16*)d_in[5]; const bf16* wqkv=(const bf16*)d_in[6]; const bf16* Bwo=(const bf16*)d_in[7];
    bf16* out=(bf16*)d_out; bf16* ws=(bf16*)d_ws;
    bf16* QA=ws; bf16* KA=QA+2048*512; bf16* VA=KA+2048*16; bf16* OA=VA+2048*16; bf16* QKV=OA+2048*512; bf16* OB=QKV+2048*1536;
    gemm_naive<<<dim3(8,512),256,0,stream>>>(Ax,wq,QA,2048,512,512,512);
    gemm_naive<<<dim3(1,512),256,0,stream>>>(Ax,wk,KA,2048,16,512,16);
    gemm_naive<<<dim3(1,512),256,0,stream>>>(Ax,wv,VA,2048,16,512,16);
    gemm_naive<<<dim3(24,512),256,0,stream>>>(Bx,wqkv,QKV,2048,1536,512,1536);
    attnA_naive<<<256,256,0,stream>>>(QA,KA,VA,OA);
    attnB_naive<<<256,256,0,stream>>>(QKV,OB);
    gemm_naive<<<dim3(8,512),256,0,stream>>>(OA,Awo,out,2048,512,512,1024);
    gemm_naive<<<dim3(8,512),256,0,stream>>>(OB,Bwo,out+512,2048,512,512,1024);
}
```

```cpp
#include <hip/hip_runtime.h>
#include <hip/hip_bf16.h>
#include <stdint.h>

typedef short bf16x8 __attribute__((ext_vector_type(8)));
typedef short s16x4 __attribute__((ext_vector_type(4)));
typedef float f32x16 __attribute__((ext_vector_type(16)));
typedef float f32x4 __attribute__((ext_vector_type(4)));
typedef unsigned u32x4 __attribute__((ext_vector_type(4)));
typedef unsigned u32x2 __attribute__((ext_vector_type(2)));
typedef __bf16 bf16x2_t __attribute__((ext_vector_type(2)));
typedef float f32x2_t __attribute__((ext_vector_type(2)));
typedef short v4i16_t __attribute__((ext_vector_type(4)));
typedef unsigned short u16;

#define LAS __attribute__((address_space(3)))
#define GAS __attribute__((address_space(1)))
#define DI __device__ __forceinline__
#define MFMA32(a, b, c) __builtin_amdgcn_mfma_f32_32x32x16_bf16((a), (b), (c), 0, 0, 0)
#define MFMA16(a, b, c) __builtin_amdgcn_mfma_f32_16x16x32_bf16((a), (b), (c), 0, 0, 0)
#define SBAR() __builtin_amdgcn_sched_barrier(0)
#define PINV(x) asm volatile("" : "+v"(x))
#define RAWBAR() asm volatile("s_barrier" ::: "memory")
#define DRAIN_VM() asm volatile("s_waitcnt vmcnt(0)" ::: "memory")

constexpr unsigned WS_EPOCH = 0;
constexpr unsigned WS_TMO = 128;
constexpr unsigned WS_FLAG_KV = 4096;
constexpr unsigned WS_FLAG_A = 8192;
constexpr unsigned WS_FLAG_B = 12288;
constexpr unsigned WS_KV = 16384;
constexpr unsigned WS_OA = WS_KV + 131072;
constexpr unsigned WS_OB = WS_OA + 2097152;
constexpr unsigned WS_STAMP = WS_OB + 2097152;
constexpr unsigned WS_TOTAL = WS_STAMP + 512 * 16 * 8;
constexpr int SC1 = 16;
#define POLLSLEEP 1
#define BPRIO 0
#define KVAUX 16

constexpr int LDS_BYTES = 81920;
constexpr int XCH = 65536;
constexpr int VBLK = 272, SLOT = 8192 + 32 * VBLK;
constexpr int QPITCH = 400;
constexpr float QSCALE = 0.25f * 1.4426950408889634f;

#define STAMP(i) do { } while (0)
#define CSTAMP(i) do { } while (0)

typedef __amdgpu_buffer_rsrc_t rsrc_t;

DI unsigned cvtpk(float lo, float hi) { f32x2_t v = {lo, hi}; bf16x2_t r = __builtin_convertvector(v, bf16x2_t); return __builtin_bit_cast(unsigned, r); }
DI bf16x8 as_frag(u32x4 v) { return __builtin_bit_cast(bf16x8, v); }
DI s16x4 vtr(const LAS char* p) { return __builtin_bit_cast(s16x4, __builtin_amdgcn_ds_read_tr16_b64_v4i16((LAS v4i16_t*)p)); }
DI bf16x8 trfrag(const LAS char* p0, const LAS char* p1) { s16x4 a = vtr(p0), b = vtr(p1); return __builtin_shufflevector(a, b, 0, 1, 2, 3, 4, 5, 6, 7); }
DI u32x4 ldg16(const void* p) { return *(const GAS u32x4*)p; }
DI float dot2bf(unsigned a, unsigned b, float c) { return __builtin_amdgcn_fdot2_f32_bf16(__builtin_bit_cast(bf16x2_t, a), __builtin_bit_cast(bf16x2_t, b), c, false); }
DI float swapsum(float x) { auto r = __builtin_amdgcn_permlane32_swap(__float_as_uint(x), __float_as_uint(x), false, false); return __uint_as_float(r[0]) + __uint_as_float(r[1]); }
DI float swapmax(float x) { auto r = __builtin_amdgcn_permlane32_swap(__float_as_uint(x), __float_as_uint(x), false, false); return fmaxf(__uint_as_float(r[0]), __uint_as_float(r[1])); }

DI void poll_flags(rsrc_t rs, unsigned off, bool act, unsigned tag) {
    const unsigned long long t0 = __builtin_amdgcn_s_memrealtime();
    for (;;) {
        unsigned v = tag;
        if (act) v = __builtin_amdgcn_raw_buffer_load_b32(rs, off, 0, SC1);
        if (__all(v == tag)) break;
        __builtin_amdgcn_s_sleep(POLLSLEEP);
        if (__builtin_amdgcn_s_memrealtime() - t0 > 2000000ull) {
            if ((threadIdx.x & 63) == 0) __builtin_amdgcn_raw_buffer_store_b32(1u, rs, WS_TMO, 0, SC1);
            break;
        }
    }
    asm volatile("" ::: "memory");
}

DI void stage_w64(const u16* W, int ldw, int col0, LAS char* lds, int wave, int lane) {
    const int chunk = (lane & 7) ^ (((lane >> 4) & 1) << 2);
#pragma unroll
    for (int i = 0; i < 8; ++i) {
        const int row = (wave * 8 + i) * 8 + (lane >> 3);
        const u16* g = W + (size_t)row * ldw + col0 + chunk * 8;
        __builtin_amdgcn_global_load_lds((const GAS void*)g, (LAS void*)(lds + (wave * 8 + i) * 1024), 16, 0, 0);
    }
}

template <bool PERMK>
DI f32x16 gemm64(const LAS char* lds, const u32x4 (&bfr)[16], int nh, int kh, int lane) {
    const int h = lane >> 5, blk = (lane >> 4) & 1, q = (lane & 15) >> 2, p = lane & 3;
    const int chunk = (nh * 4 + blk * 2 + (p >> 1)) ^ (((q >> 1) & 1) << 2);
    const LAS char* base = lds + (kh * 256 + (PERMK ? 4 * h : 8 * h) + q) * 128 + chunk * 16 + (p & 1) * 8;
    constexpr int TOFF = PERMK ? 1024 : 512;
    f32x16 acc = {0};
#pragma unroll
    for (int s = 0; s < 16; ++s) {
        bf16x8 a = trfrag(base + s * 2048, base + s * 2048 + TOFF);
        acc = MFMA32(a, as_frag(bfr[s]), acc);
    }
    return acc;
}

DI void xch_reduce(LAS char* lds, const f32x16& acc, int wave, int kh, int lane, float (&v)[8]) {
    LAS f32x4* mine = (LAS f32x4*)(lds + XCH + wave * 2048);
    const LAS f32x4* theirs = (const LAS f32x4*)(lds + XCH + (wave ^ 1) * 2048);
    if (kh == 0) {
        mine[lane] = (f32x4){acc[8], acc[9], acc[10], acc[11]};
        mine[64 + lane] = (f32x4){acc[12], acc[13], acc[14], acc[15]};
    } else {
        mine[lane] = (f32x4){acc[0], acc[1], acc[2], acc[3]};
        mine[64 + lane] = (f32x4){acc[4], acc[5], acc[6], acc[7]};
    }
    __syncthreads();
    const f32x4 a = theirs[lane], b = theirs[64 + lane];
    if (kh == 0) {
#pragma unroll
        for (int j = 0; j < 4; ++j) { v[j] = acc[j] + a[j]; v[4 + j] = acc[4 + j] + b[j]; }
    } else {
#pragma unroll
        for (int j = 0; j < 4; ++j) { v[j] = acc[8 + j] + a[j]; v[4 + j] = acc[12 + j] + b[j]; }
    }
}

DI void out_tile(LAS char* lds, rsrc_t rs, unsigned ws_o, u16* out, int rb, int colbase, int wave, int lane) {
    const int kh = wave & 1, ph = (wave >> 1) & 1, nh = wave >> 2;
    const int h = lane >> 5, r = lane & 31;
    u32x4 bfr[16];
    const unsigned obase = ws_o + (unsigned)((rb * 32 + kh * 16) * 64 + ph * 32 + r) * 32u + h * 16u;
#pragma unroll
    for (int s = 0; s < 16; ++s) bfr[s] = __builtin_amdgcn_raw_buffer_load_b128(rs, obase + s * 2048u, 0, SC1);
    f32x16 acc = gemm64<true>(lds, bfr, nh, kh, lane);
    float v[8];
    xch_reduce(lds, acc, wave, kh, lane, v);
    unsigned w0 = cvtpk(v[0], v[1]), w1 = cvtpk(v[2], v[3]), w2 = cvtpk(v[4], v[5]), w3 = cvtpk(v[6], v[7]);
    auto r0 = __builtin_amdgcn_permlane32_swap(w0, w2, false, false);
    auto r1 = __builtin_amdgcn_permlane32_swap(w1, w3, false, false);
    u32x4 o = {r0[0], r1[0], r0[1], r1[1]};
    u16* dst = out + (size_t)(rb * 64 + ph * 32 + r) * 1024 + colbase + nh * 32 + kh * 16 + h * 8;
    *(GAS u32x4*)dst = o;
}

DI void role_a(const u16* xA, const u16* wq, char* ws, rsrc_t rs, LAS char* lds, int b) {
    const int tid = threadIdx.x, lane = tid & 63, wave = __builtin_amdgcn_readfirstlane(tid >> 6);
    const int rb = (b & 7) + 8 * ((b >> 3) & 3), hg = b >> 5;
    const int r = lane & 31, h = lane >> 5;
    unsigned tag = 0;
    if (wave == 0) tag = __builtin_amdgcn_raw_buffer_load_b32(rs, WS_EPOCH, 0, SC1) + 1u;
    STAMP(0);
    const int kh = wave & 1, ph = (wave >> 1) & 1, hp = wave >> 2;
    bf16x8 qf;
    {
        stage_w64(wq, 512, hg * 64, lds, wave, lane);
        u32x4 bfr[16];
        const u16* xr = xA + (size_t)(rb * 64 + ph * 32 + r) * 512 + kh * 256 + h * 8;
#pragma unroll
        for (int s = 0; s < 16; ++s) bfr[s] = ldg16(xr + s * 16);
        DRAIN_VM();
        __syncthreads();
        STAMP(1);
        f32x16 acc = gemm64<false>(lds, bfr, hp, kh, lane);
        float v[8];
        xch_reduce(lds, acc, wave, kh, lane, v);
        u32x4 qw = {cvtpk(v[0] * QSCALE, v[1] * QSCALE), cvtpk(v[2] * QSCALE, v[3] * QSCALE), cvtpk(v[4] * QSCALE, v[5] * QSCALE), cvtpk(v[6] * QSCALE, v[7] * QSCALE)};
        qf = as_frag(qw);
    }
    const int head = hg * 4 + hp * 2 + kh;
    STAMP(2);
    if (wave == 0) poll_flags(rs, WS_FLAG_KV + lane * 16, true, tag);
    __syncthreads();
    STAMP(3);
    if (tid < 96) *(LAS u32x4*)(lds + (tid >> 5) * SLOT + 8192 + (tid & 31) * VBLK + 256) = (u32x4){0x3f803f80u, 0x3f803f80u, 0x3f803f80u, 0x3f803f80u};
    if (tid == 0) *(LAS unsigned*)(lds + XCH) = 0u;
    const int kw = tid * 16, vw = 8192 + (tid >> 4) * VBLK + (tid & 15) * 16;
    const int kr = h * 512 + r * 16, vr = 8192 + h * VBLK + (r < 16 ? r : 16) * 16;
    const unsigned gk = WS_KV + tid * 16;
    f32x16 o;
    float shift = 0.f;
    for (int attempt = 0;; ++attempt) {
        u32x4 sk, sv;
        {
            u32x4 k0 = __builtin_amdgcn_raw_buffer_load_b128(rs, gk, 0, KVAUX), v0 = __builtin_amdgcn_raw_buffer_load_b128(rs, gk + 8192, 0, KVAUX);
            u32x4 k1 = __builtin_amdgcn_raw_buffer_load_b128(rs, gk + 16384, 0, KVAUX), v1 = __builtin_amdgcn_raw_buffer_load_b128(rs, gk + 16384 + 8192, 0, KVAUX);
            sk = __builtin_amdgcn_raw_buffer_load_b128(rs, gk + 32768, 0, KVAUX); sv = __builtin_amdgcn_raw_buffer_load_b128(rs, gk + 32768 + 8192, 0, KVAUX);
            *(LAS u32x4*)(lds + kw) = k0; *(LAS u32x4*)(lds + vw) = v0;
            *(LAS u32x4*)(lds + SLOT + kw) = k1; *(LAS u32x4*)(lds + SLOT + vw) = v1;
        }
        __syncthreads();
        STAMP(4); CSTAMP(12);
        bf16x8 kn = *(const LAS bf16x8*)(lds + kr + 1024);
        bf16x8 v0 = *(const LAS bf16x8*)(lds + vr);
        f32x16 cinit, sc;
        {
            const bf16x8 kf = *(const LAS bf16x8*)(lds + kr);
            if (attempt == 0) {
                const f32x16 z = {0};
                const f32x16 s = MFMA32(kf, qf, z);
                float m = fmaxf(fmaxf(s[0], s[1]), s[2]);
#pragma unroll
                for (int i = 3; i < 15; i += 2) m = fmaxf(fmaxf(m, s[i]), s[i + 1]);
                m = swapmax(fmaxf(m, s[15]));
                shift = m + 60.f;
            }
#pragma unroll
            for (int i = 0; i < 16; ++i) cinit[i] = -shift;
            sc = MFMA32(kf, qf, cinit);
        }
        o = (f32x16){0};
        bf16x8 v1p = v0; u32x4 p1p = {0u, 0u, 0u, 0u};
        int s_cur = 0, s_nxt = SLOT, s_wr = 2 * SLOT;
#define EX(i) __builtin_amdgcn_exp2f(sc[i])
        for (int c = 0; c < 8; ++c) {
            const LAS char* kc = lds + s_cur + kr; const LAS char* vc = lds + s_cur + vr;
            const LAS char* kx = lds + s_nxt + kr; const LAS char* vx = lds + s_nxt + vr;
#pragma unroll
            for (int t = 0; t < 8; ++t) {
                f32x16 sn; bf16x8 nv0, v1, kn2; u32x4 p0, p1;
                float e0 = EX(0), e1 = EX(1);
                PINV(e0); PINV(e1);
                SBAR();
                o = MFMA32(v1p, as_frag(p1p), o); PINV(o);
                v1 = *(const LAS bf16x8*)(vc + t * 4 * VBLK + 2 * VBLK);
                SBAR();
                float e2 = EX(2), e3 = EX(3);
                p0[0] = cvtpk(e0, e1);
                float e4 = EX(4), e5 = EX(5);
                p0[1] = cvtpk(e2, e3);
                float e6 = EX(6);
                PINV(p0); PINV(e4); PINV(e5); PINV(e6);
                SBAR();
                sn = MFMA32(kn, qf, cinit); PINV(sn);
                kn2 = (t < 6) ? *(const LAS bf16x8*)(kc + (t + 2) * 1024) : *(const LAS bf16x8*)(kx + (t - 6) * 1024);
                SBAR();
                float e7 = EX(7);
                p0[2] = cvtpk(e4, e5);
                float e8 = EX(8);
                p0[3] = cvtpk(e6, e7);
                float e9 = EX(9), e10 = EX(10), e11 = EX(11);
                PINV(p0); PINV(e8); PINV(e9); PINV(e10); PINV(e11);
                SBAR();
                o = MFMA32(v0, as_frag(p0), o); PINV(o);
                nv0 = (t < 7) ? *(const LAS bf16x8*)(vc + (t + 1) * 4 * VBLK) : *(const LAS bf16x8*)(vx);
                SBAR();
                p1[0] = cvtpk(e8, e9);
                float e12 = EX(12), e13 = EX(13);
                p1[1] = cvtpk(e10, e11);
                float e14 = EX(14), e15 = EX(15);
                p1[2] = cvtpk(e12, e13);
                PINV(e14); PINV(e15); PINV(p1);
                SBAR();
                p1[3] = cvtpk(e14, e15);
                PINV(p1);
                SBAR();
                v1p = v1; p1p = p1; sc = sn; v0 = nv0; kn = kn2;
                if (t == 2) {
                    RAWBAR();
                    if (c + 2 < 8) { *(LAS u32x4*)(lds + s_wr + kw) = sk; *(LAS u32x4*)(lds + s_wr + vw) = sv; }
                    if (c + 3 < 8) { sk = __builtin_amdgcn_raw_buffer_load_b128(rs, gk + (c + 3) * 16384, 0, KVAUX); sv = __builtin_amdgcn_raw_buffer_load_b128(rs, gk + (c + 3) * 16384 + 8192, 0, KVAUX); }
                    SBAR();
                }
            }
            const int tmp = s_cur; s_cur = s_nxt; s_nxt = s_wr; s_wr = tmp;
        }
#undef EX
        o = MFMA32(v1p, as_frag(p1p), o);
        const float l = o[8];
        bool ok = (l >= 8.271806e-25f) && (l <= 1.2676506e30f);
#pragma unroll
        for (int j = 0; j < 8; ++j) ok = ok && (fabsf(o[j]) <= 3.0e38f);
        if (attempt == 1) break;
        if (!__all(ok) && lane == 0) *(LAS volatile unsigned*)(lds + XCH) = 1u;
        __syncthreads();
        const unsigned anybad = *(LAS volatile unsigned*)(lds + XCH);
        if (!anybad) break;
        float m = -INFINITY;
        for (int t = 0; t < 64; ++t) {
            const u32x4 kf = __builtin_amdgcn_raw_buffer_load_b128(rs, WS_KV + (t >> 3) * 16384 + (t & 7) * 1024 + kr, 0, SC1);
            const f32x16 z = {0};
            const f32x16 s = MFMA32(as_frag(kf), qf, z);
#pragma unroll
            for (int i = 0; i < 16; ++i) m = fmaxf(m, s[i]);
        }
        shift = swapmax(m);
        __syncthreads();
    }
    STAMP(5); CSTAMP(13);
    {
        const float rl = 1.0f / o[8];
        u32x4 ow = {cvtpk(o[0] * rl, o[1] * rl), cvtpk(o[2] * rl, o[3] * rl), cvtpk(o[4] * rl, o[5] * rl), cvtpk(o[6] * rl, o[7] * rl)};
        __builtin_amdgcn_raw_buffer_store_b128(ow, rs, WS_OA + (unsigned)((rb * 32 + head) * 64 + ph * 32 + r) * 32u + h * 16u, 0, SC1);
    }
    DRAIN_VM();
    __syncthreads();
    if (tid == 0) __builtin_amdgcn_raw_buffer_store_b32(tag, rs, WS_FLAG_A + (rb * 8 + hg) * 16, 0, SC1);
    STAMP(6);
    if (b == 0 && wave == 0) {
#pragma unroll 1
        for (int i = 0; i < 8; ++i) poll_flags(rs, WS_FLAG_A + (i * 64 + lane) * 16, true, tag);
        if (lane == 0) __builtin_amdgcn_raw_buffer_store_b32(tag, rs, WS_EPOCH, 0, SC1);
    }
}

DI void role_b(const u16* xA, const u16* wk, const u16* wv, const u16* woA, const u16* xB, const u16* wqkv, const u16* woB, u16* out,
               char* ws, rsrc_t rs, LAS char* lds, int b) {
    const int tid = threadIdx.x, lane = tid & 63, wave = __builtin_amdgcn_readfirstlane(tid >> 6);
    const int rb = (b & 7) + 8 * ((b >> 3) & 3), hg = b >> 5;
    const int r = lane & 31, h = lane >> 5;
    unsigned tag = 0;
    if (wave == 0) tag = __builtin_amdgcn_raw_buffer_load_b32(rs, WS_EPOCH, 0, SC1) + 1u;
    __builtin_amdgcn_s_setprio(BPRIO);
    STAMP(0);
    if (b < 64) {
#pragma unroll
        for (int i = 0; i < 4; ++i) {
            const int piece = wave * 4 + i;
            const u16* g = (piece < 16 ? wk + piece * 512 : wv + (piece - 16) * 512) + lane * 8;
            __builtin_amdgcn_global_load_lds((const GAS void*)g, (LAS void*)(lds + piece * 1024), 16, 0, 0);
        }
        u32x4 xf[16];
        const int which = wave & 1, s16 = (wave >> 1) & 1, g = lane >> 4, c16 = lane & 15;
        if (wave < 4) {
            const u16* xr = xA + (size_t)(b * 32 + s16 * 16 + c16) * 512 + g * 8;
#pragma unroll
            for (int s = 0; s < 16; ++s) xf[s] = ldg16(xr + s * 32);
        }
        DRAIN_VM();
        __syncthreads();
        if (wave < 4) {
            const LAS char* base = lds + which * 16384 + (8 * g + (c16 >> 2)) * 32 + (lane & 3) * 8;
            f32x4 acc = {0, 0, 0, 0};
            const unsigned cbase = WS_KV + (b >> 3) * 16384;
            const int t = b & 7;
            unsigned off;
            if (which == 0) {
#pragma unroll
                for (int s = 0; s < 16; ++s) acc = MFMA16(trfrag(base + s * 1024, base + s * 1024 + 128), as_frag(xf[s]), acc);
                off = cbase + t * 1024 + (g & 1) * 512 + (s16 * 16 + c16) * 16 + (g >> 1) * 8;
            } else {
#pragma unroll
                for (int s = 0; s < 16; ++s) acc = MFMA16(as_frag(xf[s]), trfrag(base + s * 1024, base + s * 1024 + 128), acc);
                off = cbase + 8192 + ((t * 2 + s16) * 2 + (g & 1)) * 256 + c16 * 16 + (g >> 1) * 8;
            }
            u32x2 w = {cvtpk(acc[0], acc[1]), cvtpk(acc[2], acc[3])};
            __builtin_amdgcn_raw_buffer_store_b64(w, rs, off, 0, SC1);
        }
        DRAIN_VM();
        __syncthreads();
        if (tid == 0) __builtin_amdgcn_raw_buffer_store_b32(tag, rs, WS_FLAG_KV + b * 16, 0, SC1);
    }
    STAMP(1);
    {
        const int rt = (wave < 2) ? 0 : (wave < 5 ? 1 : 2);
        const int seg = (wave < 2) ? wave + 1 : (wave < 5 ? wave - 2 : wave - 5);
        int xrow = rb * 64 - 32 + rt * 32 + r; xrow = xrow < 0 ? 0 : xrow;
        const u16* xr = xB + (size_t)xrow * 512 + h * 8;
        const int wrow = tid >> 3, wch = tid & 7;
        const u16* wg = wqkv + (size_t)wrow * 1536 + hg * 64 + wch * 8;
        const int wl = wrow * 128 + ((wch ^ (((wrow >> 1) & 1) << 2)) << 4);
        const int blk = (lane >> 4) & 1, q = (lane & 15) >> 2, p = lane & 3;
        const int ch0 = (blk * 2 + (p >> 1)) ^ (((q >> 1) & 1) << 2);
        const int rdbase = seg * 8192 + (8 * h + q) * 128 + (p & 1) * 8;
        const int rd0 = rdbase + ch0 * 16, rd1 = rdbase + (ch0 ^ 4) * 16;
        u32x4 wst[3], wst2[3], xa[4], xb[4];
#pragma unroll
        for (int i = 0; i < 3; ++i) wst[i] = ldg16(wg + i * 512);
#pragma unroll
        for (int s = 0; s < 4; ++s) xa[s] = ldg16(xr + s * 16);
#pragma unroll
        for (int i = 0; i < 3; ++i) wst2[i] = ldg16(wg + (size_t)64 * 1536 + i * 512);
#pragma unroll
        for (int s = 0; s < 4; ++s) xb[s] = ldg16(xr + 64 + s * 16);
#pragma unroll
        for (int i = 0; i < 3; ++i) *(LAS u32x4*)(lds + i * 8192 + wl) = wst[i];
        __syncthreads();
        f32x16 acc0 = {0}, acc1 = {0};
#pragma unroll
        for (int k = 0; k < 8; k += 2) {
#pragma unroll
            for (int i = 0; i < 3; ++i) *(LAS u32x4*)(lds + 24576 + i * 8192 + wl) = wst2[i];
            if (k + 2 < 8) {
#pragma unroll
                for (int i = 0; i < 3; ++i) wst[i] = ldg16(wg + (size_t)(k + 2) * 64 * 1536 + i * 512);
            }
#pragma unroll
            for (int s = 0; s < 4; ++s) {
                acc0 = MFMA32(trfrag(lds + rd0 + s * 2048, lds + rd0 + s * 2048 + 512), as_frag(xa[s]), acc0);
                acc1 = MFMA32(trfrag(lds + rd1 + s * 2048, lds + rd1 + s * 2048 + 512), as_frag(xa[s]), acc1);
            }
            if (k + 2 < 8) {
#pragma unroll
                for (int s = 0; s < 4; ++s) xa[s] = ldg16(xr + (k + 2) * 64 + s * 16);
            }
            __syncthreads();
            if (k + 2 < 8) {
#pragma unroll
                for (int i = 0; i < 3; ++i) *(LAS u32x4*)(lds + i * 8192 + wl) = wst[i];
            }
            if (k + 3 < 8) {
#pragma unroll
                for (int i = 0; i < 3; ++i) wst2[i] = ldg16(wg + (size_t)(k + 3) * 64 * 1536 + i * 512);
            }
#pragma unroll
            for (int s = 0; s < 4; ++s) {
                acc0 = MFMA32(trfrag(lds + 24576 + rd0 + s * 2048, lds + 24576 + rd0 + s * 2048 + 512), as_frag(xb[s]), acc0);
                acc1 = MFMA32(trfrag(lds + 24576 + rd1 + s * 2048, lds + 24576 + rd1 + s * 2048 + 512), as_frag(xb[s]), acc1);
            }
            if (k + 3 < 8) {
#pragma unroll
                for (int s = 0; s < 4; ++s) xb[s] = ldg16(xr + (k + 3) * 64 + s * 16);
            }
            __syncthreads();
        }
        STAMP(2);
        LAS char* qrow = lds + (rt * 32 + r) * QPITCH + seg * 128 + h * 16;
        *(LAS u32x4*)(qrow + 0)  = (u32x4){cvtpk(acc0[0], acc0[1]), cvtpk(acc0[2], acc0[3]), cvtpk(acc0[4], acc0[5]), cvtpk(acc0[6], acc0[7])};
        *(LAS u32x4*)(qrow + 32) = (u32x4){cvtpk(acc0[8], acc0[9]), cvtpk(acc0[10], acc0[11]), cvtpk(acc0[12], acc0[13]), cvtpk(acc0[14], acc0[15])};
        *(LAS u32x4*)(qrow + 64) = (u32x4){cvtpk(acc1[0], acc1[1]), cvtpk(acc1[2], acc1[3]), cvtpk(acc1[4], acc1[5]), cvtpk(acc1[6], acc1[7])};
        *(LAS u32x4*)(qrow + 96) = (u32x4){cvtpk(acc1[8], acc1[9]), cvtpk(acc1[10], acc1[11]), cvtpk(acc1[12], acc1[13]), cvtpk(acc1[14], acc1[15])};
        __syncthreads();
    }
    {
        const int hd = wave >> 1, pos = (wave & 1) * 32 + r;
        const LAS char* row = lds + (32 + pos) * QPITCH + hd * 32 + h * 16;
        const u32x4 qv = *(const LAS u32x4*)(row);
        float sc[8];
#pragma unroll
        for (int w = 0; w < 8; ++w) {
            const u32x4 kv = *(const LAS u32x4*)(row + 128 - 2 * w * QPITCH);
            float s = 0.f;
#pragma unroll
            for (int j = 0; j < 4; ++j) s = dot2bf(qv[j], kv[j], s);
            s = swapsum(s);
            sc[w] = (rb * 64 + pos - 2 * w >= 0) ? s * QSCALE : -INFINITY;
        }
        float m = sc[0];
#pragma unroll
        for (int w = 1; w < 8; ++w) m = fmaxf(m, sc[w]);
        float l = 0.f, o[8];
#pragma unroll
        for (int j = 0; j < 8; ++j) o[j] = 0.f;
#pragma unroll
        for (int w = 0; w < 8; ++w) {
            const float pw = __builtin_amdgcn_exp2f(sc[w] - m);
            l += pw;
            const u32x4 vv = *(const LAS u32x4*)(row + 256 - 2 * w * QPITCH);
#pragma unroll
            for (int j = 0; j < 4; ++j) {
                o[2 * j] = fmaf(pw, __uint_as_float(vv[j] << 16), o[2 * j]);
                o[2 * j + 1] = fmaf(pw, __uint_as_float(vv[j] & 0xffff0000u), o[2 * j + 1]);
            }
        }
        const float rl = 1.0f / l;
        u32x4 ow = {cvtpk(o[0] * rl, o[1] * rl), cvtpk(o[2] * rl, o[3] * rl), cvtpk(o[4] * rl, o[5] * rl), cvtpk(o[6] * rl, o[7] * rl)};
        __builtin_amdgcn_raw_buffer_store_b128(ow, rs, WS_OB + (unsigned)((rb * 32 + hg * 4 + hd) * 64 + pos) * 32u + h * 16u, 0, SC1);
    }
    DRAIN_VM();
    __syncthreads();
    if (tid == 0) __builtin_amdgcn_raw_buffer_store_b32(tag, rs, WS_FLAG_B + (rb * 8 + hg) * 16, 0, SC1);
    STAMP(3);
    stage_w64(woB, 512, hg * 64, lds, wave, lane);
    if (wave == 0) poll_flags(rs, WS_FLAG_B + (rb * 8 + (lane & 7)) * 16, lane < 8, tag);
    DRAIN_VM();
    __syncthreads();
    STAMP(4);
    out_tile(lds, rs, WS_OB, out, rb, 512 + hg * 64, wave, lane);
    __syncthreads();
    STAMP(5);
    stage_w64(woA, 512, hg * 64, lds, wave, lane);
    if (wave == 0) poll_flags(rs, WS_FLAG_A + (rb * 8 + (lane & 7)) * 16, lane < 8, tag);
    DRAIN_VM();
    __syncthreads();
    STAMP(6);
    out_tile(lds, rs, WS_OA, out, rb, hg * 64, wave, lane);
    STAMP(7);
}

__global__ void __launch_bounds__(512, 4) fused_kernel(const u16* xA, const u16* wq, const u16* wk, const u16* wv, const u16* woA,
                                                       const u16* xB, const u16* wqkv, const u16* woB, u16* out, char* ws) {
    extern __shared__ __attribute__((aligned(16))) char smem[];
    LAS char* lds = (LAS char*)smem;
    const rsrc_t rs = __builtin_amdgcn_make_buffer_rsrc(ws, 0, WS_TOTAL, 0x00020000);
    const int b = blockIdx.x;
    if (b < 256) role_a(xA, wq, ws, rs, lds, b);
    else role_b(xA, wk, wv, woA, xB, wqkv, woB, out, ws, rs, lds, b - 256);
}

extern "C" void kernel_launch(void* const* d_in, const int* in_sizes, int n_in, void* d_out, int out_size, void* d_ws, size_t ws_size,
                              hipStream_t stream) {
    (void)in_sizes; (void)n_in; (void)out_size; (void)ws_size;
    (void)hipFuncSetAttribute((const void*)fused_kernel, hipFuncAttributeMaxDynamicSharedMemorySize, LDS_BYTES);
    fused_kernel<<<512, 512, LDS_BYTES, stream>>>((const u16*)d_in[0], (const u16*)d_in[1], (const u16*)d_in[2], (const u16*)d_in[3], (const u16*)d_in[4],
                                                  (const u16*)d_in[5], (const u16*)d_in[6], (const u16*)d_in[7], (u16*)d_out, (char*)d_ws);
}
```

```cpp
#include <hip/hip_runtime.h>
#include <hip/hip_bf16.h>
#include <stdint.h>

typedef short bf16x8 __attribute__((ext_vector_type(8)));
typedef short s16x4 __attribute__((ext_vector_type(4)));
typedef float f32x16 __attribute__((ext_vector_type(16)));
typedef float f32x4 __attribute__((ext_vector_type(4)));
typedef unsigned u32x4 __attribute__((ext_vector_type(4)));
typedef unsigned u32x2 __attribute__((ext_vector_type(2)));
typedef __bf16 bf16x2_t __attribute__((ext_vector_type(2)));
typedef float f32x2_t __attribute__((ext_vector_type(2)));
typedef short v4i16_t __attribute__((ext_vector_type(4)));
typedef unsigned short u16;

#define LAS __attribute__((address_space(3)))
#define GAS __attribute__((address_space(1)))
#define DI __device__ __forceinline__
#define MFMA32(a, b, c) __builtin_amdgcn_mfma_f32_32x32x16_bf16((a), (b), (c), 0, 0, 0)
#define MFMA16(a, b, c) __builtin_amdgcn_mfma_f32_16x16x32_bf16((a), (b), (c), 0, 0, 0)
#define SBAR() __builtin_amdgcn_sched_barrier(0)
#define PINV(x) asm volatile("" : "+v"(x))
#define RAWBAR() asm volatile("s_barrier" ::: "memory")
#define LDSBAR() asm volatile("s_waitcnt lgkmcnt(0)\n\ts_barrier" ::: "memory")
#define DRAIN_VM() asm volatile("s_waitcnt vmcnt(0)" ::: "memory")

constexpr int VBLK = 288, SLOT = 8192 + 32 * VBLK;
constexpr unsigned WS_EPOCH = 0;
constexpr unsigned WS_TMO = 128;
constexpr unsigned FSTR = 128;
constexpr unsigned WS_FLAG_KV = 4096;
constexpr unsigned WS_FLAG_A = WS_FLAG_KV + 256 * FSTR;
constexpr unsigned WS_FLAG_B = WS_FLAG_A + 256 * FSTR;
constexpr unsigned WS_KV = WS_FLAG_B + 256 * FSTR;
constexpr unsigned WS_OA = WS_KV + 8 * SLOT;
constexpr unsigned WS_OB = WS_OA + 2097152;
constexpr unsigned WS_STAMP = WS_OB + 2097152;
constexpr unsigned WS_TOTAL = WS_STAMP + 65536 + 256 * 12 * 32;
constexpr int SC1 = 16;
#define POLLSLEEP 2
#define BPRIO 3

constexpr int LDS_BYTES = 163840;
constexpr int RB = 4 * SLOT;
constexpr int XCH = 65536;
constexpr int SYNC = RB + 81920;
constexpr int QIMG = SYNC + 1024;
enum { S_CONS = 0, S_LAND = 8, S_PRE = 16, S_BCNT = 20, S_ACNT = 24, S_PUB = 28, S_DONE1 = 32, S_AEND = 48 };
constexpr int QPITCH = 400;
constexpr float QSCALE = 0.25f * 1.4426950408889634f;

#define STAMP(i) do { } while (0)
#define CSTAMP(i) do { } while (0)
#define STAMPDUMP() do { } while (0)

typedef __amdgpu_buffer_rsrc_t rsrc_t;

DI unsigned cvtpk(float lo, float hi) { f32x2_t v = {lo, hi}; bf16x2_t r = __builtin_convertvector(v, bf16x2_t); return __builtin_bit_cast(unsigned, r); }
DI bf16x8 as_frag(u32x4 v) { return __builtin_bit_cast(bf16x8, v); }
DI s16x4 vtr(const LAS char* p) { return __builtin_bit_cast(s16x4, __builtin_amdgcn_ds_read_tr16_b64_v4i16((LAS v4i16_t*)p)); }
DI bf16x8 trfrag(const LAS char* p0, const LAS char* p1) { s16x4 a = vtr(p0), b = vtr(p1); return __builtin_shufflevector(a, b, 0, 1, 2, 3, 4, 5, 6, 7); }
DI u32x4 ldg16(const void* p) { return *(const GAS u32x4*)p; }
DI float dot2bf(unsigned a, unsigned b, float c) { return __builtin_amdgcn_fdot2_f32_bf16(__builtin_bit_cast(bf16x2_t, a), __builtin_bit_cast(bf16x2_t, b), c, false); }
DI float swapsum(float x) { auto r = __builtin_amdgcn_permlane32_swap(__float_as_uint(x), __float_as_uint(x), false, false); return __uint_as_float(r[0]) + __uint_as_float(r[1]); }
DI float swapmax(float x) { auto r = __builtin_amdgcn_permlane32_swap(__float_as_uint(x), __float_as_uint(x), false, false); return fmaxf(__uint_as_float(r[0]), __uint_as_float(r[1])); }
DI void dma16(unsigned voff, const void* base, unsigned lds_base) {
    unsigned keep;
    asm volatile("s_mov_b32 %0, m0\n\ts_mov_b32 m0, %3\n\ts_nop 0\n\tglobal_load_lds_dwordx4 %1, %2\n\ts_mov_b32 m0, %0"
                 : "=&s"(keep) : "v"(voff), "s"(base), "s"(lds_base) : "memory");
}
DI void dma16_sc1(unsigned voff, const void* base, unsigned lds_base) {
    unsigned keep;
    asm volatile("s_mov_b32 %0, m0\n\ts_mov_b32 m0, %3\n\ts_nop 0\n\tglobal_load_lds_dwordx4 %1, %2 sc1\n\ts_mov_b32 m0, %0"
                 : "=&s"(keep) : "v"(voff), "s"(base), "s"(lds_base) : "memory");
}
DI void dma4(unsigned voff, const void* base, unsigned lds_base) {
    unsigned keep;
    asm volatile("s_mov_b32 %0, m0\n\ts_mov_b32 m0, %3\n\ts_nop 0\n\tglobal_load_lds_dwordx4 %1, %2\n\tglobal_load_lds_dwordx4 %1, %2 offset:1024\n\t"
                 "global_load_lds_dwordx4 %1, %2 offset:2048\n\tglobal_load_lds_dwordx4 %1, %2 offset:3072\n\ts_mov_b32 m0, %0"
                 : "=&s"(keep) : "v"(voff), "s"(base), "s"(lds_base) : "memory");
}
DI void dma4_sc1(unsigned voff, const void* base, unsigned lds_base) {
    unsigned keep;
    asm volatile("s_mov_b32 %0, m0\n\ts_mov_b32 m0, %3\n\ts_nop 0\n\tglobal_load_lds_dwordx4 %1, %2 sc1\n\tglobal_load_lds_dwordx4 %1, %2 offset:1024 sc1\n\t"
                 "global_load_lds_dwordx4 %1, %2 offset:2048 sc1\n\tglobal_load_lds_dwordx4 %1, %2 offset:3072 sc1\n\ts_mov_b32 m0, %0"
                 : "=&s"(keep) : "v"(voff), "s"(base), "s"(lds_base) : "memory");
}
#define PACE 1
#define PACEB() do { __builtin_amdgcn_s_sleep(PACE); SBAR(); } while (0)
DI unsigned rfl(unsigned x) { return __builtin_amdgcn_readfirstlane(x); }
template <int OFF> DI void sig(unsigned a) { asm volatile("s_mov_b64 exec, 1\n\tds_add_u32 %0, %1 offset:%c2\n\ts_mov_b64 exec, -1" :: "v"(a), "v"(1u), "i"(OFF) : "memory"); }
template <int OFF> DI unsigned sig_rtn(unsigned a, unsigned val) {
    unsigned old;
    asm volatile("s_mov_b64 exec, 1\n\tds_add_rtn_u32 %0, %1, %2 offset:%c3\n\ts_waitcnt lgkmcnt(0)\n\ts_mov_b64 exec, -1" : "=&v"(old) : "v"(a), "v"(val), "i"(OFF) : "memory");
    return rfl(old);
}
template <int OFF> DI unsigned peek(unsigned a) { unsigned v; asm volatile("ds_read_b32 %0, %1 offset:%c2\n\ts_waitcnt lgkmcnt(0)" : "=&v"(v) : "v"(a), "i"(OFF) : "memory"); return rfl(v); }
template <int SLP, int OFF> DI void await(unsigned a, unsigned target) {
    unsigned v, sv, cnt;
    asm volatile("s_mov_b32 %2, 0x100000\n"
                 "1:\n\tds_read_b32 %0, %3 offset:%c6\n\ts_waitcnt lgkmcnt(0)\n\tv_readfirstlane_b32 %1, %0\n\ts_nop 0\n\ts_cmp_ge_u32 %1, %4\n\ts_cbranch_scc1 2f\n\t"
                 "s_sleep %5\n\ts_sub_u32 %2, %2, 1\n\ts_cbranch_scc0 1b\n"
                 "2:"
                 : "=&v"(v), "=&s"(sv), "=&s"(cnt) : "v"(a), "s"(rfl(target)), "i"(SLP), "i"(OFF) : "memory", "scc");
}
template <int OFF> DI void gbar(unsigned a, unsigned& gen, unsigned n) {
    asm volatile("s_waitcnt lgkmcnt(0)" ::: "memory");
    gen += n;
    sig<OFF>(a);
    await<0, OFF>(a, gen);
}

DI bool timed_out(rsrc_t rs, unsigned long long t0) {
    if (__builtin_amdgcn_s_memrealtime() - t0 > 2000000ull) {
        if ((threadIdx.x & 63) == 0) __builtin_amdgcn_raw_buffer_store_b32(1u, rs, WS_TMO, 0, SC1);
        return true;
    }
    return false;
}
DI void poll_flags(rsrc_t rs, unsigned off, bool act, unsigned tag) {
    const unsigned long long t0 = __builtin_amdgcn_s_memrealtime();
    for (;;) {
        unsigned v = tag;
        if (act) v = __builtin_amdgcn_raw_buffer_load_b32(rs, off, 0, SC1);
        if (__all(v == tag)) break;
        __builtin_amdgcn_s_sleep(POLLSLEEP);
        if (timed_out(rs, t0)) break;
    }
    asm volatile("" ::: "memory");
}
DI void poll_flags4(rsrc_t rs, unsigned off, unsigned tag) {
    const unsigned long long t0 = __builtin_amdgcn_s_memrealtime();
    for (;;) {
        const u32x4 v = __builtin_amdgcn_raw_buffer_load_b128(rs, off, 0, SC1);
        if (__all(v[0] == tag && v[1] == tag && v[2] == tag && v[3] == tag)) break;
        __builtin_amdgcn_s_sleep(POLLSLEEP);
        if (timed_out(rs, t0)) break;
    }
    asm volatile("" ::: "memory");
}

template <int NW>
DI void stage_w64(const u16* W, int col0, unsigned dst, int wave, int lane) {
    constexpr int NP = 64 / NW;
    const int rl = lane >> 3;
    const int ch0 = (lane & 7) ^ (((rl >> 1) & 1) << 2);
    const unsigned v0 = (unsigned)(((wave * NP * 8 + rl) * 512 + col0 + ch0 * 8) * 2);
#pragma unroll
    for (int i = 0; i < NP; ++i) dma16(v0, (const char*)W + i * 8192, rfl(dst + (wave * NP + i) * 1024));
}

DI void out_load(rsrc_t rs, unsigned ws_o, int rb, int wave, int lane, u32x4 (&bfr)[16]) {
    const unsigned obase = ws_o + (unsigned)((rb * 32 + (wave & 1) * 16) * 64 + (wave >> 1) * 32 + (lane & 31)) * 32u + (lane >> 5) * 16u;
#pragma unroll
    for (int s = 0; s < 16; ++s) bfr[s] = __builtin_amdgcn_raw_buffer_load_b128(rs, obase + s * 2048u, 0, SC1);
    SBAR();
}
DI f32x16 out_mma(LAS char* lds, const u32x4 (&bfr)[16], int wave, int lane) {
    const int kh = wave & 1, h = lane >> 5;
    const int blk = (lane >> 4) & 1, q = (lane & 15) >> 2, p = lane & 3;
    const int ch0 = (blk * 2 + (p >> 1)) ^ (((q >> 1) & 1) << 2);
    const LAS char* rowp = lds + (kh * 256 + 4 * h + q) * 128 + (p & 1) * 8;
    const LAS char* b0 = rowp + ch0 * 16; const LAS char* b1 = rowp + (ch0 ^ 4) * 16;
    f32x16 acc0 = {0}, acc1 = {0};
    bf16x8 w0[3], w1[3];
#pragma unroll
    for (int s = 0; s < 2; ++s) { w0[s] = trfrag(b0 + s * 2048, b0 + s * 2048 + 1024); w1[s] = trfrag(b1 + s * 2048, b1 + s * 2048 + 1024); }
    SBAR();
#pragma unroll
    for (int s = 0; s < 16; ++s) {
        if (s + 2 < 16) { w0[(s + 2) % 3] = trfrag(b0 + (s + 2) * 2048, b0 + (s + 2) * 2048 + 1024); w1[(s + 2) % 3] = trfrag(b1 + (s + 2) * 2048, b1 + (s + 2) * 2048 + 1024); }
        SBAR();
        acc0 = MFMA32(w0[s % 3], as_frag(bfr[s]), acc0);
        acc1 = MFMA32(w1[s % 3], as_frag(bfr[s]), acc1);
        SBAR();
    }
    LAS f32x4* mine = (LAS f32x4*)(lds + XCH + wave * 4096);
    if (kh == 0) {
#pragma unroll
        for (int i = 0; i < 4; ++i) mine[i * 64 + lane] = (f32x4){acc1[4 * i], acc1[4 * i + 1], acc1[4 * i + 2], acc1[4 * i + 3]};
        return acc0;
    }
#pragma unroll
    for (int i = 0; i < 4; ++i) mine[i * 64 + lane] = (f32x4){acc0[4 * i], acc0[4 * i + 1], acc0[4 * i + 2], acc0[4 * i + 3]};
    return acc1;
}
DI void out_store(LAS char* lds, const f32x16& keep, u16* out, int rb, int colbase, int wave, int lane) {
    const int kh = wave & 1, ph = wave >> 1, h = lane >> 5, r = lane & 31;
    const LAS f32x4* theirs = (const LAS f32x4*)(lds + XCH + (wave ^ 1) * 4096);
    unsigned w[8];
#pragma unroll
    for (int i = 0; i < 4; ++i) {
        const f32x4 t = theirs[i * 64 + lane];
        w[2 * i] = cvtpk(keep[4 * i] + t[0], keep[4 * i + 1] + t[1]); w[2 * i + 1] = cvtpk(keep[4 * i + 2] + t[2], keep[4 * i + 3] + t[3]);
    }
    u16* dst = out + (size_t)(rb * 64 + ph * 32 + r) * 1024 + colbase + kh * 32 + h * 8;
#pragma unroll
    for (int i = 0; i < 2; ++i) {
        auto r0 = __builtin_amdgcn_permlane32_swap(w[4 * i], w[4 * i + 2], false, false);
        auto r1 = __builtin_amdgcn_permlane32_swap(w[4 * i + 1], w[4 * i + 3], false, false);
        *(GAS u32x4*)(dst + i * 16) = (u32x4){r0[0], r1[0], r0[1], r1[1]};
    }
}

DI void issue_chunk(const char* kvg, int c, unsigned dst, int i4, int lane) {
    const char* src = kvg + c * SLOT;
    dma4_sc1(lane * 16, src + i4 * 4096, rfl(dst + i4 * 4096));
    if (i4 == 0) dma16_sc1(lane * 16, src + 16384, rfl(dst + 16384));
}
DI void issue_xrow(const u16* xt, int row, unsigned dst, int lane) {
    dma16((unsigned)((lane ^ (row & 15)) << 4), (const char*)xt + row * 1024, rfl(dst + row * 1024));
}

DI float tilemax(const f32x16& s) {
    float m = fmaxf(fmaxf(s[0], s[1]), s[2]);
#pragma unroll
    for (int i = 3; i < 15; i += 2) m = fmaxf(fmaxf(m, s[i]), s[i + 1]);
    return fmaxf(m, s[15]);
}
DI void role_a(const u16* xA, const u16* wq, char* ws, rsrc_t rs, LAS char* lds, int b, int rb, int hg, int wave) {
    const int lane = threadIdx.x & 63;
    const int r = lane & 31, h = lane >> 5;
    const int qp = wave & 3, kh = wave >> 2;
    const unsigned lds0 = (unsigned)(uintptr_t)lds;
    const unsigned sy = lds0 + SYNC;
    const unsigned syk = sy + 4 * kh;
    const char* kvg = ws + WS_KV;
    unsigned tagv;
    STAMP(0);
    if (wave == 0 && lane < 16) *(LAS unsigned*)(lds + SYNC + lane * 4) = 0u;
    LDSBAR();
    {
        const u16* xt = xA + (size_t)rb * 64 * 512;
        const int kv0 = (hg >> 1) * 16;
#pragma unroll
        for (int i = 0; i < 6; ++i) { const int row = wave * 6 + i; issue_xrow(xt, row < kv0 ? row : row + 16, lds0 + RB, lane); }
        stage_w64<8>(wq, hg * 64, lds0, wave, lane);
        tagv = __builtin_amdgcn_raw_buffer_load_b32(rs, WS_EPOCH, 0, SC1);
        SBAR();
        DRAIN_VM();
        RAWBAR();
        STAMP(1);
        if (wave < 4) {
            const int hp = wave >> 1, ph = wave & 1;
            const int blk = (lane >> 4) & 1, q = (lane & 15) >> 2, p = lane & 3;
            const LAS char* pa = lds + (8 * h + q) * 128 + (((hp * 4 + blk * 2 + (p >> 1)) ^ (((q >> 1) & 1) << 2)) << 4) + (p & 1) * 8;
            const LAS char* xrow = lds + RB + (ph * 32 + r) * 1024;
            const LAS char* xp[8];
#pragma unroll
            for (int i = 0; i < 8; ++i) xp[i] = xrow + (((2 * i + h) ^ (r & 15)) << 4);
            f32x16 acc = {0};
            constexpr int PD = 4;
            bf16x8 xf[PD + 1], wf[PD + 1];
#pragma unroll
            for (int s = 0; s < PD; ++s) { xf[s] = *(const LAS bf16x8*)(xp[s & 7] + (s >> 3) * 256); wf[s] = trfrag(pa + s * 2048, pa + s * 2048 + 512); }
            SBAR();
#pragma unroll
            for (int s = 0; s < 32; ++s) {
                if (s + PD < 32) { xf[(s + PD) % (PD + 1)] = *(const LAS bf16x8*)(xp[(s + PD) & 7] + ((s + PD) >> 3) * 256); wf[(s + PD) % (PD + 1)] = trfrag(pa + (s + PD) * 2048, pa + (s + PD) * 2048 + 512); }
                SBAR();
                acc = MFMA32(wf[s % (PD + 1)], xf[s % (PD + 1)], acc);
                SBAR();
            }
            LAS char* dst = lds + QIMG + (hp * 128 + ph * 32 + r) * 32 + h * 16;
            *(LAS u32x4*)(dst) = (u32x4){cvtpk(acc[0] * QSCALE, acc[1] * QSCALE), cvtpk(acc[2] * QSCALE, acc[3] * QSCALE), cvtpk(acc[4] * QSCALE, acc[5] * QSCALE), cvtpk(acc[6] * QSCALE, acc[7] * QSCALE)};
            *(LAS u32x4*)(dst + 64 * 32) = (u32x4){cvtpk(acc[8] * QSCALE, acc[9] * QSCALE), cvtpk(acc[10] * QSCALE, acc[11] * QSCALE), cvtpk(acc[12] * QSCALE, acc[13] * QSCALE), cvtpk(acc[14] * QSCALE, acc[15] * QSCALE)};
        }
        STAMP(2);
        LDSBAR();
        STAMP(3);
    }
    const unsigned tag = rfl(tagv) + 1u;
    const unsigned sA = (unsigned)(2 * kh) * SLOT, sB = sA + SLOT;
    issue_chunk(kvg, 4 * kh, lds0 + sA, qp, lane);
    bf16x8 qfa = *(const LAS bf16x8*)(lds + QIMG + (qp * 64 + r) * 32 + h * 16);
    bf16x8 qfb = *(const LAS bf16x8*)(lds + QIMG + (qp * 64 + 32 + r) * 32 + h * 16);
    DRAIN_VM();
    RAWBAR();
    const int kr = h * 512 + r * 16, vr = 8192 + h * VBLK + (r < 17 ? r : 17) * 16;
    f32x16 oa, ob;
    float sha = 0.f, shb = 0.f;
    unsigned agen = 0;
    for (int attempt = 0;; ++attempt) {
        STAMP(4); CSTAMP(12);
        bf16x8 kc = *(const LAS bf16x8*)(lds + sA + kr);
        bf16x8 kn = *(const LAS bf16x8*)(lds + sA + kr + 1024);
        bf16x8 v0 = *(const LAS bf16x8*)(lds + sA + vr);
        bf16x8 v1 = *(const LAS bf16x8*)(lds + sA + vr + 2 * VBLK);
        f32x16 cia, cib, sc;
        if (attempt == 0) {
            const bf16x8 k0 = *(const LAS bf16x8*)(lds + kr);
            const f32x16 z = {0};
            sha = swapmax(tilemax(MFMA32(k0, qfa, z))) + 60.f;
            shb = swapmax(tilemax(MFMA32(k0, qfb, z))) + 60.f;
        }
#pragma unroll
        for (int i = 0; i < 16; ++i) { cia[i] = -sha; cib[i] = -shb; }
        sc = MFMA32(kc, qfa, cia);
        oa = (f32x16){0}; ob = (f32x16){0};
        bf16x8 v1p = v1; u32x4 pp = {0u, 0u, 0u, 0u};
        unsigned s_cur = sA, s_nxt = sB;
#define EX(i) __builtin_amdgcn_exp2f(sc[i])
#define VTILE(PEND, NEXT, MINE, MID) do { \
            f32x16 sn; u32x4 p0, p1; \
            float e0 = EX(0), e1 = EX(1); PINV(e0); PINV(e1); SBAR(); \
            PEND; SBAR(); \
            float e2 = EX(2), e3 = EX(3); p0[0] = cvtpk(e0, e1); float e4 = EX(4), e5 = EX(5); p0[1] = cvtpk(e2, e3); float e6 = EX(6); \
            PINV(p0); PINV(e4); PINV(e5); PINV(e6); SBAR(); \
            sn = NEXT; PINV(sn); SBAR(); \
            float e7 = EX(7); p0[2] = cvtpk(e4, e5); float e8 = EX(8); p0[3] = cvtpk(e6, e7); PINV(p0); PINV(e8); SBAR(); \
            float e9 = EX(9), e10 = EX(10), e11 = EX(11); PINV(e9); PINV(e10); PINV(e11); SBAR(); \
            MINE; SBAR(); \
            MID; \
            float e12 = EX(12); p1[0] = cvtpk(e8, e9); float e13 = EX(13); p1[1] = cvtpk(e10, e11); float e14 = EX(14), e15 = EX(15); \
            p1[2] = cvtpk(e12, e13); p1[3] = cvtpk(e14, e15); PINV(p1); SBAR(); \
            pp = p1; sc = sn; } while (0)
        for (int j = 0; j < 4; ++j) {
            const LAS char* kcp = lds + s_cur + kr; const LAS char* vcp = lds + s_cur + vr;
            const LAS char* kxp = lds + s_nxt + kr; const LAS char* vxp = lds + s_nxt + vr;
#pragma unroll
            for (int t = 0; t < 8; ++t) {
                bf16x8 k2, nv0, nv1;
                if (t == 0) __builtin_amdgcn_s_setprio(2); else if (t == 3) __builtin_amdgcn_s_setprio(1); else if (t == 6) __builtin_amdgcn_s_setprio(0);
                VTILE({ ob = MFMA32(v1p, as_frag(pp), ob); PINV(ob); }, MFMA32(kc, qfb, cib), { oa = MFMA32(v0, as_frag(p0), oa); PINV(oa); },
                      { k2 = (t < 6) ? *(const LAS bf16x8*)(kcp + (t + 2) * 1024) : *(const LAS bf16x8*)(kxp + (t - 6) * 1024); SBAR();
                        nv1 = (t < 7) ? *(const LAS bf16x8*)(vcp + (t + 1) * 4 * VBLK + 2 * VBLK) : *(const LAS bf16x8*)(vxp + 2 * VBLK); SBAR();
                        nv0 = (t < 7) ? *(const LAS bf16x8*)(vcp + (t + 1) * 4 * VBLK) : *(const LAS bf16x8*)(vxp); SBAR(); });
                VTILE({ oa = MFMA32(v1, as_frag(pp), oa); PINV(oa); }, MFMA32(kn, qfa, cia), { ob = MFMA32(v0, as_frag(p0), ob); PINV(ob); }, { });
                v1p = v1; v1 = nv1; v0 = nv0; kc = kn; kn = k2;
                if (t == 0) { if (j >= 1) sig<S_CONS>(syk); SBAR(); }
                if (t == 1) {
                    if (j == 1 || j == 2) {
                        await<0, S_CONS>(syk, 12 * attempt + 4 * j);
                        issue_chunk(kvg, 4 * kh + j + 1, lds0 + s_nxt, qp, lane);
                    }
                    SBAR();
                }
                if (t == 4) { if (j == 1 || j == 2) { DRAIN_VM(); sig<S_LAND>(syk); } SBAR(); }
                if (t == 5) {
                    if (j == 0) { if (attempt == 0) await<0, S_PRE>(sy, 4); }
                    else if (j < 3) await<0, S_LAND>(syk, 8 * attempt + 4 * j);
                    SBAR();
                }
            }
            const unsigned tmp = s_cur; s_cur = s_nxt; s_nxt = tmp;
        }
        ob = MFMA32(v1p, as_frag(pp), ob);
#undef EX
#undef VTILE
        sig<S_AEND>(sy);
        STAMP(9);
        int lane2 = lane; PINV(lane2);
        const int r2 = lane2 & 31, h2 = lane2 >> 5;
        LAS char* part = lds + 2 * SLOT + qp * 4352;
        if (kh == 1) {
            await<0, S_CONS>(syk, 12 * attempt + 12);
            *(LAS f32x4*)(part + lane2 * 16) = (f32x4){oa[0], oa[1], oa[2], oa[3]};
            *(LAS f32x4*)(part + 1024 + lane2 * 16) = (f32x4){oa[4], oa[5], oa[6], oa[7]};
            *(LAS f32x4*)(part + 2176 + lane2 * 16) = (f32x4){ob[0], ob[1], ob[2], ob[3]};
            *(LAS f32x4*)(part + 2176 + 1024 + lane2 * 16) = (f32x4){ob[4], ob[5], ob[6], ob[7]};
            if (h2 == 0) { *(LAS float*)(part + 2048 + r2 * 4) = oa[8]; *(LAS float*)(part + 2176 + 2048 + r2 * 4) = ob[8]; }
            sig<S_DONE1>(sy + 4 * qp);
        } else {
            await<0, S_DONE1>(sy + 4 * qp, attempt + 1);
            const f32x4 a0 = *(const LAS f32x4*)(part + lane2 * 16), a1 = *(const LAS f32x4*)(part + 1024 + lane2 * 16);
            const f32x4 b0 = *(const LAS f32x4*)(part + 2176 + lane2 * 16), b1 = *(const LAS f32x4*)(part + 2176 + 1024 + lane2 * 16);
            const float la = oa[8] + *(const LAS float*)(part + 2048 + r2 * 4), lb = ob[8] + *(const LAS float*)(part + 2176 + 2048 + r2 * 4);
            const float sa = swapsum(h2 == 0 ? la : 0.f), sb_ = swapsum(h2 == 0 ? lb : 0.f);
            const float ra = __builtin_amdgcn_rcpf(sa), rb_ = __builtin_amdgcn_rcpf(sb_);
            float amax = 0.f;
#pragma unroll
            for (int i = 0; i < 4; ++i) {
                oa[i] += a0[i]; oa[4 + i] += a1[i]; ob[i] += b0[i]; ob[4 + i] += b1[i];
            }
            const unsigned off = WS_OA + (unsigned)((rb * 32 + hg * 4 + qp) * 64 + r2) * 32u + h2 * 16u;
            __builtin_amdgcn_raw_buffer_store_b128((u32x4){cvtpk(oa[0] * ra, oa[1] * ra), cvtpk(oa[2] * ra, oa[3] * ra), cvtpk(oa[4] * ra, oa[5] * ra), cvtpk(oa[6] * ra, oa[7] * ra)}, rs, off, 0, SC1);
            __builtin_amdgcn_raw_buffer_store_b128((u32x4){cvtpk(ob[0] * rb_, ob[1] * rb_), cvtpk(ob[2] * rb_, ob[3] * rb_), cvtpk(ob[4] * rb_, ob[5] * rb_), cvtpk(ob[6] * rb_, ob[7] * rb_)}, rs, off + 1024u, 0, SC1);
            SBAR();
            bool ok = (sa >= 8.271806e-25f) && (sa <= 1.2676506e30f) && (sb_ >= 8.271806e-25f) && (sb_ <= 1.2676506e30f);
#pragma unroll
            for (int i = 0; i < 8; ++i) ok = ok && (fabsf(oa[i]) <= 3.0e38f) && (fabsf(ob[i]) <= 3.0e38f);
            (void)amax;
            const bool bad = attempt == 0 && !__all(ok);
            DRAIN_VM();
            const unsigned mine = bad ? 0x10001u : 1u;
            const unsigned tot = sig_rtn<S_PUB>(sy, mine) + mine;
            if ((tot & 0xffffu) == 4u * (attempt + 1) && (attempt == 1 || (tot >> 16) == 0)) {
                unsigned tg = tag; PINV(tg);
                if (lane2 == 0) __builtin_amdgcn_raw_buffer_store_b32(tg, rs, WS_FLAG_A + (rb * 8 + hg) * FSTR, 0, SC1);
            }
        }
        STAMP(5); CSTAMP(13);
        unsigned pub;
        for (unsigned spins = 0; ((pub = peek<S_PUB>(sy)) & 0xffffu) < 4u * (attempt + 1) && spins < (1u << 18); ++spins) __builtin_amdgcn_s_sleep(2);
        if (attempt == 1 || (pub >> 16) == 0) break;
        float ma = -INFINITY, mb = -INFINITY;
        for (int t = 0; t < 64; ++t) {
            const u32x4 kf = __builtin_amdgcn_raw_buffer_load_b128(rs, WS_KV + (t >> 3) * SLOT + (t & 7) * 1024 + kr, 0, SC1);
            const f32x16 z = {0};
            ma = fmaxf(ma, tilemax(MFMA32(as_frag(kf), qfa, z)));
            mb = fmaxf(mb, tilemax(MFMA32(as_frag(kf), qfb, z)));
        }
        sha = swapmax(ma); shb = swapmax(mb);
        gbar<S_ACNT>(sy, agen, 8);
        issue_chunk(kvg, 4 * kh, lds0 + sA, qp, lane); issue_chunk(kvg, 4 * kh + 1, lds0 + sB, qp, lane);
        DRAIN_VM();
        gbar<S_ACNT>(sy, agen, 8);
    }
    STAMPDUMP();
    if (b == 0 && wave == 0) {
#pragma unroll 1
        for (int i = 0; i < 8; ++i) poll_flags(rs, WS_FLAG_A + (i * 64 + lane) * FSTR, true, tag);
        if (lane == 0) __builtin_amdgcn_raw_buffer_store_b32(tag, rs, WS_EPOCH, 0, SC1);
    }
}

constexpr int B1_W = 24576, B1_X = 10240, B1_XOFF = 2 * B1_W;
DI void role_b(const u16* xA, const u16* wk, const u16* wv, const u16* woA, const u16* xB, const u16* wqkv, const u16* woB, u16* out,
               char* ws, rsrc_t rs, LAS char* ldsall, int b, int rb, int hg, int wave) {
    const int lane = threadIdx.x & 63;
    const int r = lane & 31, h = lane >> 5;
    const int g = lane >> 4, c16 = lane & 15;
    LAS char* lds = ldsall + RB;
    const unsigned lds0 = (unsigned)(uintptr_t)lds;
    const unsigned sy = (unsigned)(uintptr_t)ldsall + SYNC;
    unsigned tag = 0, gen = 0;
    __builtin_amdgcn_s_setprio(BPRIO);
    STAMP(0);
    {
        const int which = hg & 1, kv0 = (hg >> 1) * 16, t16 = rb * 4 + (hg >> 1);
        {
            const u16* W = which ? wv : wk;
            dma4(lane * 16, (const char*)W + wave * 4096, rfl(lds0 + XCH + wave * 4096));
            const u16* xt = xA + (size_t)rb * 64 * 512;
#pragma unroll
            for (int i = 0; i < 4; ++i) issue_xrow(xt, kv0 + wave * 4 + i, lds0, lane);
            tag = __builtin_amdgcn_raw_buffer_load_b32(rs, WS_EPOCH, 0, SC1);
            SBAR();
        }
        RAWBAR();
        DRAIN_VM();
        gbar<S_BCNT>(sy, gen, 4);
        tag = rfl(tag) + 1u;
        const unsigned tagk = tag;
        if (wave == 0) {
            STAMP(7);
            const LAS char* base = lds + XCH + (8 * g + (c16 >> 2)) * 32 + (lane & 3) * 8;
            const LAS char* xrow = lds + (kv0 + c16) * 1024;
            bf16x8 xf[16], wf[16];
#pragma unroll
            for (int s = 0; s < 16; ++s) { xf[s] = *(const LAS bf16x8*)(xrow + ((((4 * (s & 3) + g) ^ c16) << 4) + (s >> 2) * 256)); wf[s] = trfrag(base + s * 1024, base + s * 1024 + 128); }
            SBAR();
            f32x4 acc = {0, 0, 0, 0}, acc2 = {0, 0, 0, 0};
            const int t32 = t16 >> 1, s16 = t16 & 1;
            const unsigned cbase = WS_KV + (t32 >> 3) * SLOT;
            const int t = t32 & 7;
            if (which == 0) {
#pragma unroll
                for (int s = 0; s < 16; s += 2) { acc = MFMA16(wf[s], xf[s], acc); acc2 = MFMA16(wf[s + 1], xf[s + 1], acc2); }
                acc += acc2;
                const u32x2 w = {cvtpk(acc[0], acc[1]), cvtpk(acc[2], acc[3])};
                __builtin_amdgcn_raw_buffer_store_b64(w, rs, cbase + t * 1024 + (g & 1) * 512 + (s16 * 16 + c16) * 16 + (g >> 1) * 8, 0, SC1);
            } else {
#pragma unroll
                for (int s = 0; s < 16; s += 2) { acc = MFMA16(xf[s], wf[s], acc); acc2 = MFMA16(xf[s + 1], wf[s + 1], acc2); }
                acc += acc2;
                const u32x2 w = {cvtpk(acc[0], acc[1]), cvtpk(acc[2], acc[3])};
                const unsigned vb = cbase + 8192 + (t * 2 + s16) * 2 * VBLK;
                __builtin_amdgcn_raw_buffer_store_b64(w, rs, vb + (g & 1) * VBLK + c16 * 16 + (g >> 1) * 8, 0, SC1);
                if (lane < 8) {
                    const unsigned val = (lane & 2) ? 0u : 0x3f803f80u;
                    __builtin_amdgcn_raw_buffer_store_b64((u32x2){val, val}, rs, vb + (lane >> 2) * VBLK + 256 + (lane & 3) * 8, 0, SC1);
                }
            }
            DRAIN_VM();
            if (lane == 0) __builtin_amdgcn_raw_buffer_store_b32(tag, rs, WS_FLAG_KV + (rb * 8 + hg) * FSTR, 0, SC1);
            STAMP(8);
        }
        RAWBAR();
        poll_flags(rs, WS_FLAG_KV + (wave * 64 + lane) * FSTR, true, tagk);
        STAMP(9);
        RAWBAR();
        RAWBAR();
        issue_chunk(ws + WS_KV, 1, (unsigned)(uintptr_t)ldsall + SLOT, wave, lane);
        issue_chunk(ws + WS_KV, 5, (unsigned)(uintptr_t)ldsall + 3 * SLOT, wave, lane);
    }
    {
        const int wch = (lane & 7) ^ (((lane >> 4) & 1) << 2);
        const unsigned wvo = (unsigned)(((lane >> 3) * 1536 + hg * 64 + wch * 8) * 2);
        unsigned xvo[3];
        const int xch = (lane & 7) ^ ((4 * (wave & 1) + (lane >> 4)) & 7);
#pragma unroll
        for (int i = 0; i < 3; ++i) { int gr = rb * 64 - 16 + (wave + 4 * i) * 8 + (lane >> 3); gr = gr < 0 ? 0 : gr; gr = gr > 2047 ? 2047 : gr; xvo[i] = (unsigned)((gr * 512 + xch * 8) * 2); }
        auto issueW = [&](int k, int i) {
            const int wp = wave * 6 + i;
            dma16(wvo, (const char*)wqkv + ((size_t)(k * 64 + (wp & 7) * 8) * 1536 + (wp >> 3) * 512) * 2, rfl(lds0 + (k & 1) * B1_W + wp * 1024));
        };
        auto issueX = [&](int k, int i) {
            dma16(xvo[i], (const char*)xB + k * 128, rfl(lds0 + B1_XOFF + (k & 1) * B1_X + (wave + 4 * i) * 1024));
        };
        issueX(0, 0); issueX(0, 1); if (wave < 2) issueX(0, 2);
#pragma unroll
        for (int i = 0; i < 6; ++i) issueW(0, i);
        if (wave < 2) asm volatile("s_waitcnt vmcnt(9)" ::: "memory"); else asm volatile("s_waitcnt vmcnt(8)" ::: "memory");
        sig<S_PRE>(sy);
        STAMP(1);
        const int rtA = (wave == 0) ? 0 : (wave == 3 ? 2 : 1), rtB = (wave == 0) ? 0 : (wave == 1 ? 1 : 2);
        const int sgA = (wave == 0) ? 1 : (wave == 2 ? 2 : 0), sgB = (wave == 0) ? 2 : (wave == 2 ? 2 : 1);
        int irA = rtA * 32 - 16 + r; irA = irA < 0 ? 0 : irA;
        int irB = rtB * 32 - 16 + r; irB = irB < 0 ? 0 : irB;
        int xa[4], xb_[4];
#pragma unroll
        for (int i = 0; i < 4; ++i) { xa[i] = B1_XOFF + irA * 128 + (((2 * i + h) ^ ((irA >> 1) & 7)) << 4); xb_[i] = B1_XOFF + irB * 128 + (((2 * i + h) ^ ((irB >> 1) & 7)) << 4); }
        const int blk = (lane >> 4) & 1, q = (lane & 15) >> 2, p = lane & 3;
        const int ch0 = (blk * 2 + (p >> 1)) ^ (((q >> 1) & 1) << 2);
        const int rdl = (8 * h + q) * 128 + (p & 1) * 8;
        const int a0 = rdl + sgA * 8192 + ch0 * 16, a1 = rdl + sgA * 8192 + (ch0 ^ 4) * 16;
        const int b0 = rdl + sgB * 8192 + ch0 * 16, b1 = rdl + sgB * 8192 + (ch0 ^ 4) * 16;
        f32x16 acc0 = {0}, acc1 = {0}, acc2 = {0}, acc3 = {0};
#pragma unroll
        for (int k = 0; k < 8; ++k) {
            DRAIN_VM();
            gbar<S_BCNT>(sy, gen, 4);
            if (k + 1 < 8) {
                issueX(k + 1, 0); issueX(k + 1, 1); if (wave < 2) issueX(k + 1, 2);
#pragma unroll
                for (int i = 0; i < 6; ++i) issueW(k + 1, i);
            }
            SBAR();
            const LAS char* sb = lds + (k & 1) * B1_W;
            const LAS char* sx = lds + (k & 1) * B1_X;
#pragma unroll
            for (int hs = 0; hs < 2; ++hs) {
                bf16x8 fx[2][2], fw[2][4];
#pragma unroll
                for (int s = 0; s < 2; ++s) {
                    const int ks = 2 * hs + s;
                    fx[s][0] = *(const LAS bf16x8*)(sx + xa[ks]); fx[s][1] = *(const LAS bf16x8*)(sx + xb_[ks]);
                    fw[s][0] = trfrag(sb + a0 + ks * 2048, sb + a0 + ks * 2048 + 512); fw[s][1] = trfrag(sb + a1 + ks * 2048, sb + a1 + ks * 2048 + 512);
                    fw[s][2] = trfrag(sb + b0 + ks * 2048, sb + b0 + ks * 2048 + 512); fw[s][3] = trfrag(sb + b1 + ks * 2048, sb + b1 + ks * 2048 + 512);
                }
                SBAR();
#define BSTEP(ACC, FW, FX) do { ACC = MFMA32(FW, FX, ACC); PINV(ACC); SBAR(); PACEB(); } while (0)
                BSTEP(acc0, fw[0][0], fx[0][0]); BSTEP(acc1, fw[0][1], fx[0][0]); BSTEP(acc2, fw[0][2], fx[0][1]); BSTEP(acc3, fw[0][3], fx[0][1]);
                BSTEP(acc0, fw[1][0], fx[1][0]); BSTEP(acc1, fw[1][1], fx[1][0]); BSTEP(acc2, fw[1][2], fx[1][1]); BSTEP(acc3, fw[1][3], fx[1][1]);
#undef BSTEP
            }
        }
        gbar<S_BCNT>(sy, gen, 4);
        STAMP(2);
#define PK8(A, o) (u32x4){cvtpk(A[o], A[o + 1]), cvtpk(A[o + 2], A[o + 3]), cvtpk(A[o + 4], A[o + 5]), cvtpk(A[o + 6], A[o + 7])}
        if (rtA != 0 || r >= 16) {
            LAS char* qa = lds + (rtA * 32 - 16 + r) * QPITCH + sgA * 128 + h * 16;
            *(LAS u32x4*)(qa + 0) = PK8(acc0, 0); *(LAS u32x4*)(qa + 32) = PK8(acc0, 8); *(LAS u32x4*)(qa + 64) = PK8(acc1, 0); *(LAS u32x4*)(qa + 96) = PK8(acc1, 8);
            LAS char* qb = lds + (rtB * 32 - 16 + r) * QPITCH + sgB * 128 + h * 16;
            *(LAS u32x4*)(qb + 0) = PK8(acc2, 0); *(LAS u32x4*)(qb + 32) = PK8(acc2, 8); *(LAS u32x4*)(qb + 64) = PK8(acc3, 0); *(LAS u32x4*)(qb + 96) = PK8(acc3, 8);
        }
#undef PK8
        gbar<S_BCNT>(sy, gen, 4);
    }
#pragma unroll 1
    for (int ph = 0; ph < 2; ++ph) {
        const int pos = ph * 32 + r;
        const LAS char* row = lds + (16 + pos) * QPITCH + wave * 32;
        const u32x4 q0 = *(const LAS u32x4*)(row), q1 = *(const LAS u32x4*)(row + 16);
        const LAS char* krow = row + 128 - 8 * h * QPITCH;
        float sc[4];
#pragma unroll
        for (int i = 0; i < 4; ++i) {
            const u32x4 k0 = *(const LAS u32x4*)(krow - 2 * i * QPITCH), k1 = *(const LAS u32x4*)(krow - 2 * i * QPITCH + 16);
            float s = 0.f, s2 = 0.f;
#pragma unroll
            for (int j = 0; j < 4; ++j) { s = dot2bf(q0[j], k0[j], s); s2 = dot2bf(q1[j], k1[j], s2); }
            sc[i] = (rb * 64 + pos - 2 * (4 * h + i) >= 0) ? (s + s2) * QSCALE : -INFINITY;
        }
        const float m = swapmax(fmaxf(fmaxf(sc[0], sc[1]), fmaxf(sc[2], sc[3])));
        float pw[4];
#pragma unroll
        for (int i = 0; i < 4; ++i) pw[i] = __builtin_amdgcn_exp2f(sc[i] - m);
        const float l = swapsum((pw[0] + pw[1]) + (pw[2] + pw[3]));
        const unsigned pa = cvtpk(pw[0], pw[1]), pb = cvtpk(pw[2], pw[3]);
        const auto ra = __builtin_amdgcn_permlane32_swap(pa, pa, false, false);
        const auto rb2 = __builtin_amdgcn_permlane32_swap(pb, pb, false, false);
        const unsigned pp[4] = {ra[0], rb2[0], ra[1], rb2[1]};
        float o[8];
#pragma unroll
        for (int j = 0; j < 8; ++j) o[j] = 0.f;
        const LAS char* vrow = row + 256 + h * 16;
#pragma unroll
        for (int w2 = 0; w2 < 4; ++w2) {
            const u32x4 va = *(const LAS u32x4*)(vrow - 4 * w2 * QPITCH), vb = *(const LAS u32x4*)(vrow - (4 * w2 + 2) * QPITCH);
#pragma unroll
            for (int j = 0; j < 4; ++j) {
                o[2 * j] = dot2bf(__builtin_amdgcn_perm(vb[j], va[j], 0x05040100u), pp[w2], o[2 * j]);
                o[2 * j + 1] = dot2bf(__builtin_amdgcn_perm(vb[j], va[j], 0x07060302u), pp[w2], o[2 * j + 1]);
            }
        }
        const float rl = __builtin_amdgcn_rcpf(l);
        u32x4 ow = {cvtpk(o[0] * rl, o[1] * rl), cvtpk(o[2] * rl, o[3] * rl), cvtpk(o[4] * rl, o[5] * rl), cvtpk(o[6] * rl, o[7] * rl)};
        __builtin_amdgcn_raw_buffer_store_b128(ow, rs, WS_OB + (unsigned)((rb * 32 + hg * 4 + wave) * 64 + pos) * 32u + h * 16u, 0, SC1);
    }
    DRAIN_VM();
    gbar<S_BCNT>(sy, gen, 4);
    if (wave == 0 && lane == 0) __builtin_amdgcn_raw_buffer_store_b32(tag, rs, WS_FLAG_B + (rb * 8 + hg) * FSTR, 0, SC1);
    STAMP(3);
    u32x4 bfr[16];
    stage_w64<4>(woB, hg * 64, lds0, wave, lane);
    if (wave == 0) poll_flags(rs, WS_FLAG_B + (rb * 8 + (lane & 7)) * FSTR, lane < 8, tag);
    DRAIN_VM();
    { asm volatile("s_waitcnt lgkmcnt(0)" ::: "memory"); gen += 4; sig<S_BCNT>(sy); await<2, S_BCNT>(sy, gen); }
    out_load(rs, WS_OB, rb, wave, lane, bfr);
    STAMP(4);
    await<4, S_AEND>(sy, 8);
    {
        const f32x16 keep = out_mma(lds, bfr, wave, lane);
        gbar<S_BCNT>(sy, gen, 4);
        stage_w64<4>(woA, hg * 64, lds0, wave, lane);
        out_store(lds, keep, out, rb, 512 + hg * 64, wave, lane);
    }
    STAMP(5);
    if (wave == 0) poll_flags(rs, WS_FLAG_A + (rb * 8 + (lane & 7)) * FSTR, lane < 8, tag);
    DRAIN_VM();
    gbar<S_BCNT>(sy, gen, 4);
    STAMP(6);
    out_load(rs, WS_OA, rb, wave, lane, bfr);
    {
        const f32x16 keep = out_mma(lds, bfr, wave, lane);
        gbar<S_BCNT>(sy, gen, 4);
        out_store(lds, keep, out, rb, hg * 64, wave, lane);
    }
    STAMP(10);
    STAMPDUMP();
}

__global__ void __launch_bounds__(768, 3) fused_kernel(const u16* xA, const u16* wq, const u16* wk, const u16* wv, const u16* woA,
                                                       const u16* xB, const u16* wqkv, const u16* woB, u16* out, char* ws) {
    extern __shared__ __attribute__((aligned(16))) char smem[];
    LAS char* lds = (LAS char*)smem;
    const rsrc_t rs = __builtin_amdgcn_make_buffer_rsrc(ws, 0, WS_TOTAL, 0x00020000);
    const int b = blockIdx.x;
    const int rb = ((b & 7) >> 1) * 8 + ((b >> 3) & 7), hg = (b & 1) * 4 + (b >> 6);
    const int wave = __builtin_amdgcn_readfirstlane(threadIdx.x >> 6);
    if (wave < 8) role_a(xA, wq, ws, rs, lds, b, rb, hg, wave);
    else role_b(xA, wk, wv, woA, xB, wqkv, woB, out, ws, rs, lds, b, rb, hg, wave - 8);
}

extern "C" void kernel_launch(void* const* d_in, const int* in_sizes, int n_in, void* d_out, int out_size, void* d_ws, size_t ws_size,
                              hipStream_t stream) {
    (void)in_sizes; (void)n_in; (void)out_size; (void)ws_size;
    (void)hipFuncSetAttribute((const void*)fused_kernel, hipFuncAttributeMaxDynamicSharedMemorySize, LDS_BYTES);
    fused_kernel<<<256, 768, LDS_BYTES, stream>>>((const u16*)d_in[0], (const u16*)d_in[1], (const u16*)d_in[2], (const u16*)d_in[3], (const u16*)d_in[4],
                                                  (const u16*)d_in[5], (const u16*)d_in[6], (const u16*)d_in[7], (u16*)d_out, (char*)d_ws);
}
```

```cpp
#include <hip/hip_runtime.h>
#include <hip/hip_bf16.h>
#include <stdint.h>

typedef short bf16x8 __attribute__((ext_vector_type(8)));
typedef short s16x4 __attribute__((ext_vector_type(4)));
typedef float f32x16 __attribute__((ext_vector_type(16)));
typedef float f32x4 __attribute__((ext_vector_type(4)));
typedef unsigned u32x4 __attribute__((ext_vector_type(4)));
typedef unsigned u32x2 __attribute__((ext_vector_type(2)));
typedef __bf16 bf16x2_t __attribute__((ext_vector_type(2)));
typedef float f32x2_t __attribute__((ext_vector_type(2)));
typedef short v4i16_t __attribute__((ext_vector_type(4)));
typedef unsigned short u16;

#define LAS __attribute__((address_space(3)))
#define GAS __attribute__((address_space(1)))
#define DI __device__ __forceinline__
#define MFMA32(a, b, c) __builtin_amdgcn_mfma_f32_32x32x16_bf16((a), (b), (c), 0, 0, 0)
#define MFMA16(a, b, c) __builtin_amdgcn_mfma_f32_16x16x32_bf16((a), (b), (c), 0, 0, 0)
#define SBAR() __builtin_amdgcn_sched_barrier(0)
#define PINV(x) asm volatile("" : "+v"(x))
#define RAWBAR() asm volatile("s_barrier" ::: "memory")
#define LDSBAR() asm volatile("s_waitcnt lgkmcnt(0)\n\ts_barrier" ::: "memory")
#define DRAIN_VM() asm volatile("s_waitcnt vmcnt(0)" ::: "memory")

constexpr int VBLK = 288, SLOT = 8192 + 32 * VBLK;
constexpr unsigned WS_EPOCH = 0;
constexpr unsigned WS_TMO = 128;
constexpr unsigned FSTR = 128;
constexpr unsigned WS_FLAG_KV = 4096;
constexpr unsigned WS_FLAG_A = WS_FLAG_KV + 256 * FSTR;
constexpr unsigned WS_FLAG_B = WS_FLAG_A + 256 * FSTR;
constexpr unsigned WS_KV = WS_FLAG_B + 256 * FSTR;
constexpr unsigned WS_OA = WS_KV + 8 * SLOT;
constexpr unsigned WS_OB = WS_OA + 2097152;
constexpr unsigned WS_STAMP = WS_OB + 2097152;
constexpr unsigned WS_TOTAL = WS_STAMP + 65536 + 256 * 12 * 32;
constexpr int SC1 = 16;
#define POLLSLEEP 2
#define BPRIO 3

constexpr int LDS_BYTES = 163840;
constexpr int RB = 4 * SLOT;
constexpr int XCH = 65536;
constexpr int SYNC = RB + 81920;
constexpr int QIMG = SYNC + 1024;
enum { S_CONS = 0, S_LAND = 8, S_PRE = 16, S_BCNT = 20, S_ACNT = 24, S_PUB = 28, S_DONE1 = 32, S_AEND = 48, S_GO = 52, S_HOLD = 56, S_OBRDY = 60, S_GB2 = 64, S_XB = 68, S_VERD = 72 };
#define SHOFF 60.f
#define LMIN 8.271806e-25f
#define BSLP 2
constexpr int QPITCH = 400;
constexpr unsigned CODE_LINES = 176;
constexpr int B1_X = 10240, B1_W = 24576, B1_ST = B1_X + B1_W;
constexpr int H_KVW = B1_ST, H_KVX = B1_ST + 16384;
static_assert(2 * B1_ST <= 81920 && H_KVX + 16384 <= 2 * B1_ST, "role B's region");
constexpr float QSCALE = 0.25f * 1.4426950408889634f;

#define STAMP(i) do { } while (0)
#define CSTAMP(i) do { } while (0)
#define STAMPDUMP() do { } while (0)

typedef __amdgpu_buffer_rsrc_t rsrc_t;

DI unsigned cvtpk(float lo, float hi) { f32x2_t v = {lo, hi}; bf16x2_t r = __builtin_convertvector(v, bf16x2_t); return __builtin_bit_cast(unsigned, r); }
DI bf16x8 as_frag(u32x4 v) { return __builtin_bit_cast(bf16x8, v); }
DI s16x4 vtr(const LAS char* p) { return __builtin_bit_cast(s16x4, __builtin_amdgcn_ds_read_tr16_b64_v4i16((LAS v4i16_t*)p)); }
DI bf16x8 trfrag(const LAS char* p0, const LAS char* p1) { s16x4 a = vtr(p0), b = vtr(p1); return __builtin_shufflevector(a, b, 0, 1, 2, 3, 4, 5, 6, 7); }
DI u32x4 ldg16(const void* p) { return *(const GAS u32x4*)p; }
DI float dot2bf(unsigned a, unsigned b, float c) { return __builtin_amdgcn_fdot2_f32_bf16(__builtin_bit_cast(bf16x2_t, a), __builtin_bit_cast(bf16x2_t, b), c, false); }
DI float swapsum(float x) { auto r = __builtin_amdgcn_permlane32_swap(__float_as_uint(x), __float_as_uint(x), false, false); return __uint_as_float(r[0]) + __uint_as_float(r[1]); }
DI float swapmax(float x) { auto r = __builtin_amdgcn_permlane32_swap(__float_as_uint(x), __float_as_uint(x), false, false); return fmaxf(__uint_as_float(r[0]), __uint_as_float(r[1])); }
DI void dma16(unsigned voff, const void* base, unsigned lds_base) {
    unsigned keep;
    asm volatile("s_mov_b32 %0, m0\n\ts_mov_b32 m0, %3\n\ts_nop 0\n\tglobal_load_lds_dwordx4 %1, %2\n\ts_mov_b32 m0, %0"
                 : "=&s"(keep) : "v"(voff), "s"(base), "s"(lds_base) : "memory");
}
DI void dma16_sc1(unsigned voff, const void* base, unsigned lds_base) {
    unsigned keep;
    asm volatile("s_mov_b32 %0, m0\n\ts_mov_b32 m0, %3\n\ts_nop 0\n\tglobal_load_lds_dwordx4 %1, %2 sc1\n\ts_mov_b32 m0, %0"
                 : "=&s"(keep) : "v"(voff), "s"(base), "s"(lds_base) : "memory");
}
DI void dma4(unsigned voff, const void* base, unsigned lds_base) {
    unsigned keep;
    asm volatile("s_mov_b32 %0, m0\n\ts_mov_b32 m0, %3\n\ts_nop 0\n\tglobal_load_lds_dwordx4 %1, %2\n\tglobal_load_lds_dwordx4 %1, %2 offset:1024\n\t"
                 "global_load_lds_dwordx4 %1, %2 offset:2048\n\tglobal_load_lds_dwordx4 %1, %2 offset:3072\n\ts_mov_b32 m0, %0"
                 : "=&s"(keep) : "v"(voff), "s"(base), "s"(lds_base) : "memory");
}
DI void dma4_sc1(unsigned voff, const void* base, unsigned lds_base) {
    unsigned keep;
    asm volatile("s_mov_b32 %0, m0\n\ts_mov_b32 m0, %3\n\ts_nop 0\n\tglobal_load_lds_dwordx4 %1, %2 sc1\n\tglobal_load_lds_dwordx4 %1, %2 offset:1024 sc1\n\t"
                 "global_load_lds_dwordx4 %1, %2 offset:2048 sc1\n\tglobal_load_lds_dwordx4 %1, %2 offset:3072 sc1\n\ts_mov_b32 m0, %0"
                 : "=&s"(keep) : "v"(voff), "s"(base), "s"(lds_base) : "memory");
}
#define PACE 1
#define PACEB() do { __builtin_amdgcn_s_sleep(PACE); SBAR(); } while (0)
DI void dmag1(unsigned v0, const void* base, unsigned lds_base) {
    asm volatile("s_mov_b32 m0, %2\n\ts_nop 0\n\tglobal_load_lds_dwordx4 %0, %1" :: "v"(v0), "s"(base), "s"(lds_base) : "memory");
}
DI void dmag2(unsigned v0, unsigned v1, const void* base, unsigned lds_base) {
    asm volatile("s_mov_b32 m0, %3\n\ts_nop 0\n\tglobal_load_lds_dwordx4 %0, %2\n\tglobal_load_lds_dwordx4 %1, %2 offset:1024" :: "v"(v0), "v"(v1), "s"(base), "s"(lds_base) : "memory");
}
DI void dmag4(unsigned v0, unsigned v1, unsigned v2, unsigned v3, const void* base, unsigned lds_base) {
    asm volatile("s_mov_b32 m0, %5\n\ts_nop 0\n\tglobal_load_lds_dwordx4 %0, %4\n\tglobal_load_lds_dwordx4 %1, %4 offset:1024\n\t"
                 "global_load_lds_dwordx4 %2, %4 offset:2048\n\tglobal_load_lds_dwordx4 %3, %4 offset:3072"
                 :: "v"(v0), "v"(v1), "v"(v2), "v"(v3), "s"(base), "s"(lds_base) : "memory");
}
DI void l2_touch(unsigned voff, const void* base, unsigned lds_corner) {
    asm volatile("s_mov_b32 m0, %2\n\ts_nop 0\n\tglobal_load_lds_dword %0, %1" :: "v"(voff), "s"(base), "s"(lds_corner) : "memory");
}
DI unsigned rfl(unsigned x) { return __builtin_amdgcn_readfirstlane(x); }
template <int OFF> DI void sig(unsigned a) { asm volatile("s_mov_b64 exec, 1\n\tds_add_u32 %0, %1 offset:%c2\n\ts_mov_b64 exec, -1" :: "v"(a), "v"(1u), "i"(OFF) : "memory"); }
template <int OFF> DI unsigned sig_rtn(unsigned a, unsigned val) {
    unsigned old;
    asm volatile("s_mov_b64 exec, 1\n\tds_add_rtn_u32 %0, %1, %2 offset:%c3\n\ts_waitcnt lgkmcnt(0)\n\ts_mov_b64 exec, -1" : "=&v"(old) : "v"(a), "v"(val), "i"(OFF) : "memory");
    return rfl(old);
}
template <int OFF> DI unsigned peek(unsigned a) { unsigned v; asm volatile("ds_read_b32 %0, %1 offset:%c2\n\ts_waitcnt lgkmcnt(0)" : "=&v"(v) : "v"(a), "i"(OFF) : "memory"); return rfl(v); }
template <int SLP, int OFF> DI void await(unsigned a, unsigned target) {
    unsigned v, sv, cnt;
    asm volatile("s_mov_b32 %2, 0x100000\n"
                 "1:\n\tds_read_b32 %0, %3 offset:%c6\n\ts_waitcnt lgkmcnt(0)\n\tv_readfirstlane_b32 %1, %0\n\ts_nop 0\n\ts_cmp_ge_u32 %1, %4\n\ts_cbranch_scc1 2f\n\t"
                 "s_sleep %5\n\ts_sub_u32 %2, %2, 1\n\ts_cbranch_scc0 1b\n"
                 "2:"
                 : "=&v"(v), "=&s"(sv), "=&s"(cnt) : "v"(a), "s"(rfl(target)), "i"(SLP), "i"(OFF) : "memory", "scc");
}
template <int OFF, int SLP = 0> DI void gbar(unsigned a, unsigned& gen, unsigned n) {
    asm volatile("s_waitcnt lgkmcnt(0)" ::: "memory");
    gen += n;
    sig<OFF>(a);
    await<SLP, OFF>(a, gen);
}

DI bool timed_out(rsrc_t rs, unsigned long long t0) {
    if (__builtin_amdgcn_s_memrealtime() - t0 > 2000000ull) {
        if ((threadIdx.x & 63) == 0) __builtin_amdgcn_raw_buffer_store_b32(1u, rs, WS_TMO, 0, SC1);
        return true;
    }
    return false;
}
DI void poll_flags(rsrc_t rs, unsigned off, bool act, unsigned tag) {
    const unsigned long long t0 = __builtin_amdgcn_s_memrealtime();
    for (;;) {
        unsigned v = tag;
        if (act) v = __builtin_amdgcn_raw_buffer_load_b32(rs, off, 0, SC1);
        if (__all(v == tag)) break;
        __builtin_amdgcn_s_sleep(POLLSLEEP);
        if (timed_out(rs, t0)) break;
    }
    asm volatile("" ::: "memory");
}
DI void poll_flags4(rsrc_t rs, unsigned off, unsigned tag) {
    const unsigned long long t0 = __builtin_amdgcn_s_memrealtime();
    for (;;) {
        const u32x4 v = __builtin_amdgcn_raw_buffer_load_b128(rs, off, 0, SC1);
        if (__all(v[0] == tag && v[1] == tag && v[2] == tag && v[3] == tag)) break;
        __builtin_amdgcn_s_sleep(POLLSLEEP);
        if (timed_out(rs, t0)) break;
    }
    asm volatile("" ::: "memory");
}

template <int NW>
DI void stage_w64(const u16* W, int col0, unsigned dst, int wave, int lane) {
    constexpr int NP = 64 / NW;
    const int rl = lane >> 3;
    const int ch0 = (lane & 7) ^ (((rl >> 1) & 1) << 2);
    const unsigned v0 = (unsigned)(((wave * NP * 8 + rl) * 512 + col0 + ch0 * 8) * 2);
#pragma unroll
    for (int i = 0; i < NP; i += 4) dmag4(v0, v0 + 7168, v0 + 14336, v0 + 21504, (const char*)W + i * 8192, rfl(dst + (wave * NP + i) * 1024));
}

DI void issue_chunk(const char* kvg, int c, unsigned dst, int i4, int lane) {
    const char* src = kvg + c * SLOT;
    dma4_sc1(lane * 16, src + i4 * 4096, rfl(dst + i4 * 4096));
    if (i4 == 0) dma16_sc1(lane * 16, src + 16384, rfl(dst + 16384));
}
DI void issue_xrow(const u16* xt, int row, unsigned dst, int lane) {
    dma16((unsigned)((lane ^ (row & 15)) << 4), (const char*)xt + row * 1024, rfl(dst + row * 1024));
}

DI float tilemax(const f32x16& s) {
    float m = fmaxf(fmaxf(s[0], s[1]), s[2]);
#pragma unroll
    for (int i = 3; i < 15; i += 2) m = fmaxf(fmaxf(m, s[i]), s[i + 1]);
    return fmaxf(m, s[15]);
}
DI void role_a(const u16* xA, const u16* wq, const u16* xB, const u16* wqkv, char* ws, rsrc_t rs, LAS char* lds, int b, int rb, int hg, int wave, unsigned& tag_out) {
    const int lane = threadIdx.x & 63;
    const int r = lane & 31, h = lane >> 5;
    const int qp = wave & 3, kh = wave >> 2;
    const unsigned lds0 = (unsigned)(uintptr_t)lds;
    const unsigned sy = lds0 + SYNC;
    const unsigned syk = sy + 4 * kh;
    const char* kvg = ws + WS_KV;
    unsigned tagv;
    STAMP(0);
    if (wave == 0 && lane < 32) *(LAS unsigned*)(lds + SYNC + lane * 4) = 0u;
    LDSBAR();
    const unsigned sA = (unsigned)(2 * kh) * SLOT, sB = sA + SLOT;
    unsigned agen = 0;
    {
        tagv = __builtin_amdgcn_raw_buffer_load_b32(rs, WS_EPOCH, 0, SC1);
        SBAR();
        unsigned xv[4];
#pragma unroll
        for (int i = 0; i < 4; ++i) { const int row = wave * 8 + 2 * i + h, sl = lane & 31; xv[i] = (unsigned)((rb * 64 + row) * 1024 + (((sl & 16) | ((sl & 15) ^ (row & 15))) << 4) - i * 1024 + 4096); }
        const int wch = (lane & 7) ^ (((lane >> 4) & 1) << 2);
        const unsigned wv0 = (unsigned)((wave * 32 + (lane >> 3)) * 1024 + hg * 128 + wch * 16);
        const int hp = (wave >> 1) & 1, ph = wave & 1;
        const int blk = (lane >> 4) & 1, q = (lane & 15) >> 2, p = lane & 3;
        const LAS char* pa = lds + 32768 + (8 * h + q) * 128 + (((hp * 4 + blk * 2 + (p >> 1)) ^ (((q >> 1) & 1) << 2)) << 4) + (p & 1) * 8;
        const LAS char* xrow = lds + (ph * 32 + r) * 512;
        const LAS char* xp[8];
#pragma unroll
        for (int i = 0; i < 8; ++i) xp[i] = xrow + (((2 * i + h) ^ (r & 15)) << 4);
        f32x16 acc = {0};
        dmag4(xv[0], xv[1], xv[2], xv[3], (const char*)xA - 4096, rfl(lds0 + wave * 4096));
        dmag4(wv0, wv0 + 7168, wv0 + 14336, wv0 + 21504, (const char*)wq, rfl(lds0 + 32768 + wave * 4096));
        u32x4 hx[4], hw[4];
#pragma unroll
        for (int i = 0; i < 4; ++i) { hx[i] = ldg16((const char*)xA + 512 - 4096 + i * 1024 + xv[i]); hw[i] = ldg16((const char*)wq + 262144 + i * 8192 + wv0); }
        SBAR();
        asm volatile("s_waitcnt vmcnt(8)" ::: "memory");
        gbar<S_ACNT>(sy, agen, 8);
        STAMP(1);
#pragma unroll 1
        for (int half = 0; half < 2; ++half) {
            if (wave < 4) {
                constexpr int PD = 4;
                bf16x8 xf[PD + 1], wf[PD + 1];
#pragma unroll
                for (int s = 0; s < PD; ++s) { xf[s] = *(const LAS bf16x8*)(xp[s & 7]); wf[s] = trfrag(pa + s * 2048, pa + s * 2048 + 512); }
                SBAR();
#pragma unroll
                for (int s = 0; s < 16; ++s) {
                    if (s + PD < 16) { xf[(s + PD) % (PD + 1)] = *(const LAS bf16x8*)(xp[(s + PD) & 7] + ((s + PD) >> 3) * 256); wf[(s + PD) % (PD + 1)] = trfrag(pa + (s + PD) * 2048, pa + (s + PD) * 2048 + 512); }
                    SBAR();
                    acc = MFMA32(wf[s % (PD + 1)], xf[s % (PD + 1)], acc);
                    SBAR();
                }
            }
            if (half == 0) {
                gbar<S_ACNT, 1>(sy, agen, 8);
#pragma unroll
                for (int i = 0; i < 4; ++i) { *(LAS u32x4*)(lds + wave * 4096 + i * 1024 + lane * 16) = hx[i]; *(LAS u32x4*)(lds + 32768 + wave * 4096 + i * 1024 + lane * 16) = hw[i]; }
                gbar<S_ACNT>(sy, agen, 8);
            }
        }
        if (wave < 4) {
            acc = acc * QSCALE;
            LAS char* dst = lds + QIMG + (hp * 128 + ph * 32 + r) * 32 + h * 16;
            *(LAS u32x4*)(dst) = (u32x4){cvtpk(acc[0], acc[1]), cvtpk(acc[2], acc[3]), cvtpk(acc[4], acc[5]), cvtpk(acc[6], acc[7])};
            *(LAS u32x4*)(dst + 64 * 32) = (u32x4){cvtpk(acc[8], acc[9]), cvtpk(acc[10], acc[11]), cvtpk(acc[12], acc[13]), cvtpk(acc[14], acc[15])};
        }
        STAMP(2);
    }
    const unsigned tag = rfl(tagv) + 1u;
    if (wave >= 4) poll_flags(rs, WS_FLAG_KV + ((wave - 4) * 64 + lane) * FSTR, true, tag);
    if (wave == 4) sig<S_HOLD>(sy);
    gbar<S_ACNT, 1>(sy, agen, 8);
    STAMP(3);
    issue_chunk(kvg, 4 * kh, lds0 + sA, qp, lane);
    issue_chunk(kvg, 4 * kh + 1, lds0 + sB, qp, lane);
    bf16x8 qfa = *(const LAS bf16x8*)(lds + QIMG + (qp * 64 + r) * 32 + h * 16);
    bf16x8 qfb = *(const LAS bf16x8*)(lds + QIMG + (qp * 64 + 32 + r) * 32 + h * 16);
    if (qp == 0) asm volatile("s_waitcnt vmcnt(5)" ::: "memory"); else asm volatile("s_waitcnt vmcnt(4)" ::: "memory");
    gbar<S_ACNT>(sy, agen, 8);
    if (wave == 0) sig<S_GO>(sy);
    const int kr = h * 512 + r * 16, vr = 8192 + h * VBLK + (r < 17 ? r : 17) * 16;
    f32x16 oa, ob;
    float sha = 0.f, shb = 0.f;
    for (int attempt = 0;; ++attempt) {
        STAMP(4); CSTAMP(12);
        bf16x8 kc = *(const LAS bf16x8*)(lds + sA + kr);
        bf16x8 kn = *(const LAS bf16x8*)(lds + sA + kr + 1024);
        bf16x8 v0 = *(const LAS bf16x8*)(lds + sA + vr);
        bf16x8 v1 = *(const LAS bf16x8*)(lds + sA + vr + 2 * VBLK);
        f32x16 cia, cib, sc;
        if (attempt == 0) {
            const bf16x8 k0 = *(const LAS bf16x8*)(lds + kr);
            const f32x16 z = {0};
            sha = swapmax(tilemax(MFMA32(k0, qfa, z))) + SHOFF;
            shb = swapmax(tilemax(MFMA32(k0, qfb, z))) + SHOFF;
        }
#pragma unroll
        for (int i = 0; i < 16; ++i) { cia[i] = -sha; cib[i] = -shb; }
        sc = MFMA32(kc, qfa, cia);
        oa = (f32x16){0}; ob = (f32x16){0};
        bf16x8 v1p = v1; u32x4 pp = {0u, 0u, 0u, 0u};
        unsigned s_cur = sA, s_nxt = sB;
#define EX(i) __builtin_amdgcn_exp2f(sc[i])
#define VTILE(PEND, NEXT, MINE, MID) do { \
            f32x16 sn; u32x4 p0, p1; \
            float e0 = EX(0), e1 = EX(1); PINV(e0); PINV(e1); SBAR(); \
            PEND; SBAR(); \
            float e2 = EX(2), e3 = EX(3); p0[0] = cvtpk(e0, e1); float e4 = EX(4), e5 = EX(5); p0[1] = cvtpk(e2, e3); float e6 = EX(6); \
            PINV(p0); PINV(e4); PINV(e5); PINV(e6); SBAR(); \
            sn = NEXT; PINV(sn); SBAR(); \
            float e7 = EX(7); p0[2] = cvtpk(e4, e5); float e8 = EX(8); p0[3] = cvtpk(e6, e7); PINV(p0); PINV(e8); SBAR(); \
            float e9 = EX(9), e10 = EX(10), e11 = EX(11); PINV(e9); PINV(e10); PINV(e11); SBAR(); \
            MINE; SBAR(); \
            MID; \
            float e12 = EX(12); p1[0] = cvtpk(e8, e9); float e13 = EX(13); p1[1] = cvtpk(e10, e11); float e14 = EX(14), e15 = EX(15); \
            p1[2] = cvtpk(e12, e13); p1[3] = cvtpk(e14, e15); PINV(p1); SBAR(); \
            pp = p1; sc = sn; } while (0)
        for (int j = 0; j < 4; ++j) {
            const LAS char* kcp = lds + s_cur + kr; const LAS char* vcp = lds + s_cur + vr;
            const LAS char* kxp = lds + s_nxt + kr; const LAS char* vxp = lds + s_nxt + vr;
#pragma unroll
            for (int t = 0; t < 8; ++t) {
                bf16x8 k2, nv0, nv1;
                if (t == 0) __builtin_amdgcn_s_setprio(2); else if (t == 3) __builtin_amdgcn_s_setprio(1); else if (t == 6) __builtin_amdgcn_s_setprio(0);
                VTILE({ ob = MFMA32(v1p, as_frag(pp), ob); PINV(ob); }, MFMA32(kc, qfb, cib), { oa = MFMA32(v0, as_frag(p0), oa); PINV(oa); },
                      { k2 = (t < 6) ? *(const LAS bf16x8*)(kcp + (t + 2) * 1024) : *(const LAS bf16x8*)(kxp + (t - 6) * 1024); SBAR();
                        nv1 = (t < 7) ? *(const LAS bf16x8*)(vcp + (t + 1) * 4 * VBLK + 2 * VBLK) : *(const LAS bf16x8*)(vxp + 2 * VBLK); SBAR();
                        nv0 = (t < 7) ? *(const LAS bf16x8*)(vcp + (t + 1) * 4 * VBLK) : *(const LAS bf16x8*)(vxp); SBAR(); });
                VTILE({ oa = MFMA32(v1, as_frag(pp), oa); PINV(oa); }, MFMA32(kn, qfa, cia), { ob = MFMA32(v0, as_frag(p0), ob); PINV(ob); }, { });
                v1p = v1; v1 = nv1; v0 = nv0; kc = kn; kn = k2;
                if (t == 0) { if (j >= 1) sig<S_CONS>(syk); SBAR(); }
                if (t == 1) {
                    if (j == 1 || j == 2) {
                        await<0, S_CONS>(syk, 12 * attempt + 4 * j);
                        issue_chunk(kvg, 4 * kh + j + 1, lds0 + s_nxt, qp, lane);
                    }
                    SBAR();
                }
                if (t == 4) { if (j < 3) { DRAIN_VM(); sig<S_LAND>(syk); } SBAR(); }
                if (t == 5) {
                    if (j < 3) await<0, S_LAND>(syk, 12 * attempt + 4 * (j + 1));
                    SBAR();
                }
            }
            const unsigned tmp = s_cur; s_cur = s_nxt; s_nxt = tmp;
        }
        ob = MFMA32(v1p, as_frag(pp), ob);
#undef EX
#undef VTILE
        __builtin_amdgcn_s_setprio(3);
        sig<S_AEND>(sy);
        STAMP(9);
        int lane2 = lane; PINV(lane2);
        const int r2 = lane2 & 31, h2 = lane2 >> 5;
        LAS char* part = lds + 2 * SLOT + qp * 4352;
        if (kh == 1) {
            await<0, S_CONS>(syk, 12 * attempt + 12);
            *(LAS f32x4*)(part + lane2 * 16) = (f32x4){oa[0], oa[1], oa[2], oa[3]};
            *(LAS f32x4*)(part + 1024 + lane2 * 16) = (f32x4){oa[4], oa[5], oa[6], oa[7]};
            *(LAS f32x4*)(part + 2176 + lane2 * 16) = (f32x4){ob[0], ob[1], ob[2], ob[3]};
            *(LAS f32x4*)(part + 2176 + 1024 + lane2 * 16) = (f32x4){ob[4], ob[5], ob[6], ob[7]};
            if (h2 == 0) { *(LAS float*)(part + 2048 + r2 * 4) = oa[8]; *(LAS float*)(part + 2176 + 2048 + r2 * 4) = ob[8]; }
            sig<S_DONE1>(sy + 4 * qp);
        } else {
            await<1, S_DONE1>(sy + 4 * qp, attempt + 1);
            const f32x4 a0 = *(const LAS f32x4*)(part + lane2 * 16), a1 = *(const LAS f32x4*)(part + 1024 + lane2 * 16);
            const f32x4 b0 = *(const LAS f32x4*)(part + 2176 + lane2 * 16), b1 = *(const LAS f32x4*)(part + 2176 + 1024 + lane2 * 16);
            const float la = oa[8] + *(const LAS float*)(part + 2048 + r2 * 4), lb = ob[8] + *(const LAS float*)(part + 2176 + 2048 + r2 * 4);
            const float sa = swapsum(h2 == 0 ? la : 0.f), sb_ = swapsum(h2 == 0 ? lb : 0.f);
            const float ra = __builtin_amdgcn_rcpf(sa), rb_ = __builtin_amdgcn_rcpf(sb_);
            float amax = 0.f;
#pragma unroll
            for (int i = 0; i < 4; ++i) {
                oa[i] += a0[i]; oa[4 + i] += a1[i]; ob[i] += b0[i]; ob[4 + i] += b1[i];
            }
            const unsigned off = WS_OA + (unsigned)((rb * 32 + hg * 4 + qp) * 64 + r2) * 32u + h2 * 16u;
            __builtin_amdgcn_raw_buffer_store_b128((u32x4){cvtpk(oa[0] * ra, oa[1] * ra), cvtpk(oa[2] * ra, oa[3] * ra), cvtpk(oa[4] * ra, oa[5] * ra), cvtpk(oa[6] * ra, oa[7] * ra)}, rs, off, 0, SC1);
            __builtin_amdgcn_raw_buffer_store_b128((u32x4){cvtpk(ob[0] * rb_, ob[1] * rb_), cvtpk(ob[2] * rb_, ob[3] * rb_), cvtpk(ob[4] * rb_, ob[5] * rb_), cvtpk(ob[6] * rb_, ob[7] * rb_)}, rs, off + 1024u, 0, SC1);
            SBAR();
            bool ok = (sa >= LMIN) && (sa <= 1.2676506e30f) && (sb_ >= LMIN) && (sb_ <= 1.2676506e30f);
#pragma unroll
            for (int i = 0; i < 8; ++i) ok = ok && (fabsf(oa[i]) <= 3.0e38f) && (fabsf(ob[i]) <= 3.0e38f);
            (void)amax;
            const bool bad = attempt == 0 && !__all(ok);
            (void)sig_rtn<S_VERD>(sy, bad ? 0x10001u : 1u);
            DRAIN_VM();
            const unsigned tot = sig_rtn<S_PUB>(sy, 1u) + 1u;
            if (tot == 4u * (attempt + 1)) {
                unsigned vd;
                for (unsigned spins = 0; ((vd = peek<S_VERD>(sy)) & 0xffffu) < 4u * (attempt + 1) && spins < (1u << 18); ++spins) { }
                if (attempt == 1 || (vd >> 16) == 0) {
                    unsigned tg = tag; PINV(tg);
                    if (lane2 == 0) __builtin_amdgcn_raw_buffer_store_b32(tg, rs, WS_FLAG_A + (rb * 8 + hg) * FSTR, 0, SC1);
                }
            }
        }
        STAMP(5); CSTAMP(13);
        unsigned pub;
        for (unsigned spins = 0; ((pub = peek<S_VERD>(sy)) & 0xffffu) < 4u * (attempt + 1) && spins < (1u << 18); ++spins) __builtin_amdgcn_s_sleep(1);
        if (attempt == 1 || (pub >> 16) == 0) break;
        float ma = -INFINITY, mb = -INFINITY;
        for (int t = 0; t < 64; ++t) {
            const u32x4 kf = __builtin_amdgcn_raw_buffer_load_b128(rs, WS_KV + (t >> 3) * SLOT + (t & 7) * 1024 + kr, 0, SC1);
            const f32x16 z = {0};
            ma = fmaxf(ma, tilemax(MFMA32(as_frag(kf), qfa, z)));
            mb = fmaxf(mb, tilemax(MFMA32(as_frag(kf), qfb, z)));
        }
        sha = swapmax(ma); shb = swapmax(mb);
        gbar<S_ACNT>(sy, agen, 8);
        issue_chunk(kvg, 4 * kh, lds0 + sA, qp, lane); issue_chunk(kvg, 4 * kh + 1, lds0 + sB, qp, lane);
        DRAIN_VM();
        gbar<S_ACNT>(sy, agen, 8);
    }
    tag_out = tag;
    if (kh == 0) STAMPDUMP();
    if (b == 0 && wave == 0) {
        const unsigned long long t0 = __builtin_amdgcn_s_memrealtime();
        for (;;) {
            unsigned v[8];
#pragma unroll
            for (int i = 0; i < 8; ++i) v[i] = __builtin_amdgcn_raw_buffer_load_b32(rs, WS_FLAG_A + (i * 64 + lane) * FSTR, 0, SC1);
            bool all = true;
#pragma unroll
            for (int i = 0; i < 8; ++i) all = all && v[i] == tag;
            if (__all(all) || timed_out(rs, t0)) break;
            __builtin_amdgcn_s_sleep(2);
        }
        if (lane == 0) __builtin_amdgcn_raw_buffer_store_b32(tag, rs, WS_EPOCH, 0, SC1);
    }
}

DI void role_b(const u16* xA, const u16* wq, const u16* xB, const u16* wqkv, const u16* wk, const u16* wv, const u16* woA, const u16* woB, u16* out,
               char* ws, rsrc_t rs, LAS char* ldsall, int b, int rb, int hg, int wave, unsigned& tag_out, unsigned& gen_out) {
    const int lane = threadIdx.x & 63;
    const int r = lane & 31, h = lane >> 5;
    const int g = lane >> 4, c16 = lane & 15;
    LAS char* lds = ldsall + RB;
    const unsigned lds0 = (unsigned)(uintptr_t)lds;
    const unsigned sy = (unsigned)(uintptr_t)ldsall + SYNC;
    unsigned tag = 0, tagv, gen = 0;
    __builtin_amdgcn_s_setprio(BPRIO);
    STAMP(0);
    const int wch = (lane & 7) ^ (((lane >> 4) & 1) << 2);
    const unsigned av0 = (unsigned)(((wave & 1) * 32 + (lane >> 3)) * 3072 + (wave >> 1) * 1024 + hg * 128 + wch * 16);
    unsigned bv[4], cv;
    {
        const int che = (lane & 7) ^ ((lane >> 4) & 7), cho = (lane & 7) ^ ((4 + (lane >> 4)) & 7);
#pragma unroll
        for (int i = 0; i < 4; ++i) {
            int gr = rb * 64 - 16 + ((wave & 1) * 4 + i) * 8 + (lane >> 3); gr = gr < 0 ? 0 : gr;
            const unsigned xo = (unsigned)(gr * 1024 + ((i & 1) ? cho : che) * 16 - i * 1024 + 4096);
            const unsigned wo = (unsigned)(((wave & 1) * 32 + i * 8 + (lane >> 3)) * 3072 + 2048 + hg * 128 + wch * 16 - i * 1024);
            bv[i] = wave < 2 ? wo : xo;
        }
        cv = wave < 2 ? (unsigned)((rb * 64 - 16 + (8 + wave) * 8 + (lane >> 3)) * 1024 + (wave ? cho : che) * 16 + 4096) : bv[0];
    }
    const char* srcA = (const char*)wqkv;
    const char* srcB = wave < 2 ? (const char*)wqkv : (const char*)xB - 4096;
    const char* srcC = (const char*)xB - 4096;
    const unsigned advB = wave < 2 ? 64 * 3072 : 128;
    const unsigned dA = B1_X + wave * 4096, dB = wave < 2 ? B1_X + 16384 + wave * 4096 : (wave - 2) * 4096, dC = wave < 2 ? (8 + wave) * 1024 : dB;
    int pfr = rb * 64 - 16 + lane; pfr = pfr < 0 ? 0 : pfr;
    const unsigned pfo = wave < 3 ? (unsigned)(lane * 3072 + wave * 1024 + hg * 128) : (unsigned)(pfr * 1024);
    const unsigned pfo2 = (unsigned)((rb * 64 + 48 + (lane & 15)) * 1024);
    auto touch_step = [&](int k) {
    };
    auto issue_step = [&](unsigned buf) {
        dmag4(av0, av0 + 23552, av0 + 47104, av0 + 70656, srcA, rfl(lds0 + buf + dA));
        dmag4(bv[0], bv[1], bv[2], bv[3], srcB, rfl(lds0 + buf + dB));
        dmag1(cv, srcC, rfl(lds0 + buf + dC));
        srcA += 64 * 3072; srcB += advB; srcC += 128;
    };
    {
        const int which = hg & 1, kv0 = (hg >> 1) * 16, t16 = rb * 4 + (hg >> 1);
        const u16* xt = xA + (size_t)rb * 64 * 512;
        {
            const u16* W = which ? wv : wk;
            dma4(lane * 16, (const char*)W + wave * 4096, rfl(lds0 + H_KVW + wave * 4096));
#pragma unroll
            for (int i = 0; i < 4; ++i) { const int row = wave * 4 + i; dma16((unsigned)((lane ^ row) << 4), (const char*)xt + (kv0 + row) * 1024, rfl(lds0 + H_KVX + row * 1024)); }
            tagv = __builtin_amdgcn_raw_buffer_load_b32(rs, WS_EPOCH, 0, SC1);
            SBAR();
        }
        RAWBAR();
        DRAIN_VM();
        gbar<S_BCNT>(sy, gen, 4);
        if (wave != 0) issue_step(0);
        if (wave == 0) {
            STAMP(7);
            const LAS char* base = lds + H_KVW + (8 * g + (c16 >> 2)) * 32 + (lane & 3) * 8;
            const LAS char* xrow = lds + H_KVX + c16 * 1024;
            f32x4 acc = {0, 0, 0, 0}, acc2 = {0, 0, 0, 0};
#pragma unroll 1
            for (int s4 = 0; s4 < 4; ++s4) {
                bf16x8 xf[4], wf[4];
#pragma unroll
                for (int s = 0; s < 4; ++s) { xf[s] = *(const LAS bf16x8*)(xrow + (((4 * s + g) ^ c16) << 4) + s4 * 256); wf[s] = trfrag(base + (s4 * 4 + s) * 1024, base + (s4 * 4 + s) * 1024 + 128); }
                if (which == 0) { acc = MFMA16(wf[0], xf[0], acc); acc2 = MFMA16(wf[1], xf[1], acc2); acc = MFMA16(wf[2], xf[2], acc); acc2 = MFMA16(wf[3], xf[3], acc2); }
                else { acc = MFMA16(xf[0], wf[0], acc); acc2 = MFMA16(xf[1], wf[1], acc2); acc = MFMA16(xf[2], wf[2], acc); acc2 = MFMA16(xf[3], wf[3], acc2); }
            }
            acc += acc2;
            const u32x2 w = {cvtpk(acc[0], acc[1]), cvtpk(acc[2], acc[3])};
            const int t32 = t16 >> 1, s16 = t16 & 1, t = t32 & 7;
            const unsigned cbase = WS_KV + (t32 >> 3) * SLOT;
            const unsigned vb = cbase + 8192 + (t * 2 + s16) * 2 * VBLK;
            const unsigned off = which ? vb + (g & 1) * VBLK + c16 * 16 + (g >> 1) * 8 : cbase + t * 1024 + (g & 1) * 512 + (s16 * 16 + c16) * 16 + (g >> 1) * 8;
            __builtin_amdgcn_raw_buffer_store_b64(w, rs, off, 0, SC1);
            if (which && lane < 8) {
                const unsigned val = (lane & 2) ? 0u : 0x3f803f80u;
                __builtin_amdgcn_raw_buffer_store_b64((u32x2){val, val}, rs, vb + (lane >> 2) * VBLK + 256 + (lane & 3) * 8, 0, SC1);
            }
            DRAIN_VM();
            if (lane == 0) __builtin_amdgcn_raw_buffer_store_b32(rfl(tagv) + 1u, rs, WS_FLAG_KV + (rb * 8 + hg) * FSTR, 0, SC1);
            STAMP(8);
            issue_step(0);
        }
        tag = rfl(tagv) + 1u;
    }
    {
        const int rtA = (wave == 0) ? 0 : (wave == 3 ? 2 : 1), rtB = (wave == 0) ? 0 : (wave == 1 ? 1 : 2);
        const int sgA = (wave == 0) ? 1 : (wave == 2 ? 2 : 0), sgB = (wave == 0) ? 2 : (wave == 2 ? 2 : 1);
        int irA = rtA * 32 - 16 + r; irA = irA < 0 ? 0 : irA;
        int irB = rtB * 32 - 16 + r; irB = irB < 0 ? 0 : irB;
        int xa[4], xb_[4];
#pragma unroll
        for (int i = 0; i < 4; ++i) { xa[i] = irA * 128 + (((2 * i + h) ^ ((irA >> 1) & 7)) << 4); xb_[i] = irB * 128 + (((2 * i + h) ^ ((irB >> 1) & 7)) << 4); }
        const int blk = (lane >> 4) & 1, q = (lane & 15) >> 2, p = lane & 3;
        const int ch0 = (blk * 2 + (p >> 1)) ^ (((q >> 1) & 1) << 2);
        const int rdl = B1_X + (8 * h + q) * 128 + (p & 1) * 8;
        const int a0 = rdl + sgA * 8192 + ch0 * 16, a1 = rdl + sgA * 8192 + (ch0 ^ 4) * 16;
        const int b0 = rdl + sgB * 8192 + ch0 * 16, b1 = rdl + sgB * 8192 + (ch0 ^ 4) * 16;
        f32x16 acc0 = {0}, acc1 = {0}, acc2 = {0}, acc3 = {0};
        unsigned buf = 0;
#pragma unroll 1
        for (int k = 0; k < 8; ++k) {
            DRAIN_VM();
            gbar<S_BCNT, BSLP>(sy, gen, 4);
            bool paced = peek<S_GO>(sy) != 0;
            if (!paced && peek<S_HOLD>(sy) != 0) { await<1, S_GO>(sy, 1); paced = true; }
            if (k < 7) issue_step(B1_ST - buf);
            if (k < 6) touch_step(k + 2);
            if (k == 5) {
                l2_touch((unsigned)((wave * 128 + lane) * 1024 + hg * 128), woB, sy + 640); l2_touch((unsigned)((wave * 128 + 64 + lane) * 1024 + hg * 128), woB, sy + 640);
            }
            const LAS char* sb = lds + buf;
#pragma unroll
            for (int hs = 0; hs < 2; ++hs) {
                bf16x8 fx[2][2], fw[2][4];
#pragma unroll
                for (int s = 0; s < 2; ++s) {
                    const int ks = 2 * hs + s;
                    fx[s][0] = *(const LAS bf16x8*)(sb + xa[ks]);
                    fw[s][0] = trfrag(sb + a0 + ks * 2048, sb + a0 + ks * 2048 + 512); fw[s][1] = trfrag(sb + a1 + ks * 2048, sb + a1 + ks * 2048 + 512);
                    fx[s][1] = *(const LAS bf16x8*)(sb + xb_[ks]);
                    fw[s][2] = trfrag(sb + b0 + ks * 2048, sb + b0 + ks * 2048 + 512); fw[s][3] = trfrag(sb + b1 + ks * 2048, sb + b1 + ks * 2048 + 512);
                }
                SBAR();
#define BSTEP(ACC, FW, FX) do { ACC = MFMA32(FW, FX, ACC); PINV(ACC); SBAR(); if (paced) __builtin_amdgcn_s_sleep(PACE); SBAR(); } while (0)
                BSTEP(acc0, fw[0][0], fx[0][0]); BSTEP(acc1, fw[0][1], fx[0][0]); BSTEP(acc2, fw[0][2], fx[0][1]); BSTEP(acc3, fw[0][3], fx[0][1]);
                BSTEP(acc0, fw[1][0], fx[1][0]); BSTEP(acc1, fw[1][1], fx[1][0]); BSTEP(acc2, fw[1][2], fx[1][1]); BSTEP(acc3, fw[1][3], fx[1][1]);
#undef BSTEP
            }
            buf = B1_ST - buf;
        }
        gbar<S_BCNT>(sy, gen, 4);
        STAMP(2);
#define PK8(A, o) (u32x4){cvtpk(A[o], A[o + 1]), cvtpk(A[o + 2], A[o + 3]), cvtpk(A[o + 4], A[o + 5]), cvtpk(A[o + 6], A[o + 7])}
        if (rtA != 0 || r >= 16) {
            LAS char* qa = lds + (rtA * 32 - 16 + r) * QPITCH + sgA * 128 + h * 16;
            *(LAS u32x4*)(qa + 0) = PK8(acc0, 0); *(LAS u32x4*)(qa + 32) = PK8(acc0, 8); *(LAS u32x4*)(qa + 64) = PK8(acc1, 0); *(LAS u32x4*)(qa + 96) = PK8(acc1, 8);
            LAS char* qb = lds + (rtB * 32 - 16 + r) * QPITCH + sgB * 128 + h * 16;
            *(LAS u32x4*)(qb + 0) = PK8(acc2, 0); *(LAS u32x4*)(qb + 32) = PK8(acc2, 8); *(LAS u32x4*)(qb + 64) = PK8(acc3, 0); *(LAS u32x4*)(qb + 96) = PK8(acc3, 8);
        }
#undef PK8
        gbar<S_BCNT>(sy, gen, 4);
    }
#pragma unroll 1
    for (int ph = 0; ph < 2; ++ph) {
        const int pos = ph * 32 + r;
        const LAS char* row = lds + (16 + pos) * QPITCH + wave * 32;
        const u32x4 q0 = *(const LAS u32x4*)(row), q1 = *(const LAS u32x4*)(row + 16);
        const LAS char* krow = row + 128 - 8 * h * QPITCH;
        float sc[4];
#pragma unroll
        for (int i = 0; i < 4; ++i) {
            const u32x4 k0 = *(const LAS u32x4*)(krow - 2 * i * QPITCH), k1 = *(const LAS u32x4*)(krow - 2 * i * QPITCH + 16);
            float s = 0.f, s2 = 0.f;
#pragma unroll
            for (int j = 0; j < 4; ++j) { s = dot2bf(q0[j], k0[j], s); s2 = dot2bf(q1[j], k1[j], s2); }
            sc[i] = (rb * 64 + pos - 2 * (4 * h + i) >= 0) ? (s + s2) * QSCALE : -INFINITY;
        }
        const float m = swapmax(fmaxf(fmaxf(sc[0], sc[1]), fmaxf(sc[2], sc[3])));
        float pw[4];
#pragma unroll
        for (int i = 0; i < 4; ++i) pw[i] = __builtin_amdgcn_exp2f(sc[i] - m);
        const float l = swapsum((pw[0] + pw[1]) + (pw[2] + pw[3]));
        const unsigned pa = cvtpk(pw[0], pw[1]), pb = cvtpk(pw[2], pw[3]);
        const auto ra = __builtin_amdgcn_permlane32_swap(pa, pa, false, false);
        const auto rb2 = __builtin_amdgcn_permlane32_swap(pb, pb, false, false);
        const unsigned pp[4] = {ra[0], rb2[0], ra[1], rb2[1]};
        float o[8];
#pragma unroll
        for (int j = 0; j < 8; ++j) o[j] = 0.f;
        const LAS char* vrow = row + 256 + h * 16;
#pragma unroll
        for (int w2 = 0; w2 < 4; ++w2) {
            const u32x4 va = *(const LAS u32x4*)(vrow - 4 * w2 * QPITCH), vb = *(const LAS u32x4*)(vrow - (4 * w2 + 2) * QPITCH);
#pragma unroll
            for (int j = 0; j < 4; ++j) {
                o[2 * j] = dot2bf(__builtin_amdgcn_perm(vb[j], va[j], 0x05040100u), pp[w2], o[2 * j]);
                o[2 * j + 1] = dot2bf(__builtin_amdgcn_perm(vb[j], va[j], 0x07060302u), pp[w2], o[2 * j + 1]);
            }
        }
        const float rl = __builtin_amdgcn_rcpf(l);
        u32x4 ow = {cvtpk(o[0] * rl, o[1] * rl), cvtpk(o[2] * rl, o[3] * rl), cvtpk(o[4] * rl, o[5] * rl), cvtpk(o[6] * rl, o[7] * rl)};
        __builtin_amdgcn_raw_buffer_store_b128(ow, rs, WS_OB + (unsigned)((rb * 32 + hg * 4 + wave) * 64 + pos) * 32u + h * 16u, 0, SC1);
    }
    DRAIN_VM();
    gbar<S_BCNT>(sy, gen, 4);
    if (wave == 0 && lane == 0) __builtin_amdgcn_raw_buffer_store_b32(tag, rs, WS_FLAG_B + (rb * 8 + hg) * FSTR, 0, SC1);
    STAMP(3);
    stage_w64<4>(woB, hg * 64, lds0, wave, lane);
#pragma unroll
    for (int i = 0; i < 2; ++i)
        asm volatile("s_mov_b32 m0, %2\n\ts_nop 0\n\tglobal_load_lds_dword %0, %1" :: "v"((unsigned)(((wave * 2 + i) * 64 + lane) * 1024 + hg * 128)), "s"(woA), "s"(sy + 640) : "memory");
    DRAIN_VM();
    gbar<S_BCNT, 1>(sy, gen, 4);
    if (wave == 0) { poll_flags(rs, WS_FLAG_B + (rb * 8 + (lane & 7)) * FSTR, lane < 8, tag); sig<S_OBRDY>(sy); }
    STAMP(4);
    {
        unsigned pub;
        for (unsigned spins = 0; spins < (1u << 18); ++spins) {
            pub = peek<S_VERD>(sy);
            if ((pub & 0xffffu) >= 8u || ((pub & 0xffffu) == 4u && (pub >> 16) == 0)) break;
            __builtin_amdgcn_s_sleep(2);
        }
    }
    stage_w64<4>(woA, hg * 64, (unsigned)(uintptr_t)ldsall, wave, lane);
    DRAIN_VM();
    gbar<S_BCNT>(sy, gen, 4);
    STAMP(5);
    tag_out = tag; gen_out = gen;
}

constexpr int OPD = 6;
DI void out_tile(int br, int wave, unsigned tag, unsigned gen, u16* out, char* ws, rsrc_t rs, LAS char* ldsall, int rb, int hg) {
    const int lane = threadIdx.x & 63;
    const int r = lane & 31, h = lane >> 5;
    const unsigned sy = (unsigned)(uintptr_t)ldsall + SYNC;
    const int kh = wave & 1, ph = wave >> 1;
    const int blk = (lane >> 4) & 1, q = (lane & 15) >> 2, p = lane & 3;
    const int ch0 = (blk * 2 + (p >> 1)) ^ (((q >> 1) & 1) << 2);
    const LAS char* rowp = ldsall + (br ? RB : 0) + (kh * 256 + 4 * h + q) * 128 + (p & 1) * 8;
    const LAS char* b0 = rowp + ch0 * 16; const LAS char* b1 = rowp + (ch0 ^ 4) * 16;
    if (br) await<1, S_OBRDY>(sy, 1);
    bf16x8 w0[16], w1[16];
#pragma unroll
    for (int s = 0; s < OPD; ++s) { w0[s] = trfrag(b0 + s * 2048, b0 + s * 2048 + 1024); w1[s] = trfrag(b1 + s * 2048, b1 + s * 2048 + 1024); }
#pragma unroll
    for (int s = 0; s < OPD; ++s) { PINV(w0[s]); PINV(w1[s]); }
    if (!br) poll_flags(rs, WS_FLAG_A + (rb * 8 + (lane & 7)) * FSTR, lane < 8, tag);
    STAMP(6);
    u32x4 bfr[16];
    const unsigned obase = (br ? WS_OB : WS_OA) + (unsigned)((rb * 32 + kh * 16) * 64 + ph * 32 + r) * 32u + h * 16u;
#pragma unroll
    for (int s = 0; s < 16; ++s) bfr[s] = __builtin_amdgcn_raw_buffer_load_b128(rs, obase + s * 2048u, 0, SC1);
    SBAR();
    f32x16 acc0 = {0}, acc1 = {0};
#pragma unroll
    for (int s = 0; s < 16; ++s) {
        acc0 = MFMA32(w0[s], as_frag(bfr[s]), acc0);
        acc1 = MFMA32(w1[s], as_frag(bfr[s]), acc1);
        if (s + OPD < 16) { w0[s + OPD] = trfrag(b0 + (s + OPD) * 2048, b0 + (s + OPD) * 2048 + 1024); w1[s + OPD] = trfrag(b1 + (s + OPD) * 2048, b1 + (s + OPD) * 2048 + 1024); }
        SBAR();
    }
    LAS f32x4* mine = (LAS f32x4*)(ldsall + RB + XCH + wave * 4096);
    const LAS f32x4* theirs = (const LAS f32x4*)(ldsall + RB + XCH + (wave ^ 1) * 4096);
    const f32x16 give = kh ? acc0 : acc1, keep = kh ? acc1 : acc0;
    if (!br) await<0, S_XB>(sy, 4);
#pragma unroll
    for (int i = 0; i < 4; ++i) mine[i * 64 + lane] = (f32x4){give[4 * i], give[4 * i + 1], give[4 * i + 2], give[4 * i + 3]};
    gbar<0>(sy + (br ? S_GB2 : S_BCNT), gen, 4);
    f32x4 t[4];
#pragma unroll
    for (int i = 0; i < 4; ++i) t[i] = theirs[i * 64 + lane];
    if (br) { asm volatile("s_waitcnt lgkmcnt(0)" ::: "memory"); sig<S_XB>(sy); }
    unsigned w[8];
#pragma unroll
    for (int i = 0; i < 4; ++i) { w[2 * i] = cvtpk(keep[4 * i] + t[i][0], keep[4 * i + 1] + t[i][1]); w[2 * i + 1] = cvtpk(keep[4 * i + 2] + t[i][2], keep[4 * i + 3] + t[i][3]); }
    u16* dst = out + (size_t)(rb * 64 + ph * 32 + r) * 1024 + br * 512 + hg * 64 + kh * 32 + h * 8;
#pragma unroll
    for (int i = 0; i < 2; ++i) {
        auto r0 = __builtin_amdgcn_permlane32_swap(w[4 * i], w[4 * i + 2], false, false);
        auto r1 = __builtin_amdgcn_permlane32_swap(w[4 * i + 1], w[4 * i + 3], false, false);
        *(GAS u32x4*)(dst + i * 16) = (u32x4){r0[0], r1[0], r0[1], r1[1]};
    }
    STAMP(10);
    STAMPDUMP();
}

__global__ void __launch_bounds__(768, 3) fused_kernel(const u16* xA, const u16* wq, const u16* wk, const u16* wv, const u16* woA,
                                                       const u16* xB, const u16* wqkv, const u16* woB, u16* out, char* ws) {
    extern __shared__ __attribute__((aligned(16))) char smem[];
    LAS char* lds = (LAS char*)smem;
    const rsrc_t rs = __builtin_amdgcn_make_buffer_rsrc(ws, 0, WS_TOTAL, 0x00020000);
    const int b = blockIdx.x;
    const int rb = ((b & 7) >> 1) * 8 + ((b >> 3) & 7), hg = (b & 1) * 4 + (b >> 6);
    const int wave = __builtin_amdgcn_readfirstlane(threadIdx.x >> 6);
    unsigned tag = 0, gen = 0;
    if (wave < 8) role_a(xA, wq, xB, wqkv, ws, rs, lds, b, rb, hg, wave, tag);
    else role_b(xA, wq, xB, wqkv, wk, wv, woA, woB, out, ws, rs, lds, b, rb, hg, wave - 8, tag, gen);
    if (wave >= 4) out_tile(wave < 8 ? 1 : 0, wave & 3, tag, gen, out, ws, rs, lds, rb, hg);
}

extern "C" void kernel_launch(void* const* d_in, const int* in_sizes, int n_in, void* d_out, int out_size, void* d_ws, size_t ws_size,
                              hipStream_t stream) {
    (void)in_sizes; (void)n_in; (void)out_size; (void)ws_size;
    (void)hipFuncSetAttribute((const void*)fused_kernel, hipFuncAttributeMaxDynamicSharedMemorySize, LDS_BYTES);
    fused_kernel<<<256, 768, LDS_BYTES, stream>>>((const u16*)d_in[0], (const u16*)d_in[1], (const u16*)d_in[2], (const u16*)d_in[3], (const u16*)d_in[4],
                                                  (const u16*)d_in[5], (const u16*)d_in[6], (const u16*)d_in[7], (u16*)d_out, (char*)d_ws);
}
```

```cpp
#include <hip/hip_runtime.h>
#include <hip/hip_bf16.h>
#include <stdint.h>

typedef short bf16x8 __attribute__((ext_vector_type(8)));
typedef short s16x4 __attribute__((ext_vector_type(4)));
typedef float f32x16 __attribute__((ext_vector_type(16)));
typedef float f32x4 __attribute__((ext_vector_type(4)));
typedef unsigned u32x4 __attribute__((ext_vector_type(4)));
typedef unsigned u32x2 __attribute__((ext_vector_type(2)));
typedef __bf16 bf16x2_t __attribute__((ext_vector_type(2)));
typedef float f32x2_t __attribute__((ext_vector_type(2)));
typedef short v4i16_t __attribute__((ext_vector_type(4)));
typedef unsigned short u16;

#define LAS __attribute__((address_space(3)))
#define GAS __attribute__((address_space(1)))
#define DI __device__ __forceinline__
#define MFMA32(a, b, c) __builtin_amdgcn_mfma_f32_32x32x16_bf16((a), (b), (c), 0, 0, 0)
#define MFMA16(a, b, c) __builtin_amdgcn_mfma_f32_16x16x32_bf16((a), (b), (c), 0, 0, 0)
#define SBAR() __builtin_amdgcn_sched_barrier(0)
#define PINV(x) asm volatile("" : "+v"(x))
#define RAWBAR() asm volatile("s_barrier" ::: "memory")
#define LDSBAR() asm volatile("s_waitcnt lgkmcnt(0)\n\ts_barrier" ::: "memory")
#define DRAIN_VM() asm volatile("s_waitcnt vmcnt(0)" ::: "memory")

constexpr int VBLK = 288, SLOT = 8192 + 32 * VBLK;
constexpr unsigned WS_EPOCH = 0;
constexpr unsigned WS_TMO = 128;
constexpr unsigned FSTR = 128;
constexpr unsigned WS_FLAG_KV = 4096;
constexpr unsigned WS_FLAG_A = WS_FLAG_KV + 256 * FSTR;
constexpr unsigned WS_FLAG_B = WS_FLAG_A + 256 * FSTR;
constexpr unsigned WS_KV = WS_FLAG_B + 256 * FSTR;
constexpr unsigned WS_OA = WS_KV + 8 * SLOT;
constexpr unsigned WS_OB = WS_OA + 2097152;
constexpr unsigned WS_STAMP = WS_OB + 2097152;
constexpr unsigned WS_TOTAL = WS_STAMP + 65536 + 256 * 12 * 32;
constexpr int SC1 = 16;
#define POLLSLEEP 2
#define BPRIO 3

constexpr int LDS_BYTES = 163840;
constexpr int RB = 4 * SLOT;
constexpr int XCH = 65536;
constexpr int SYNC = RB + 81920;
constexpr int QIMG = SYNC + 1024;
enum { S_CONS = 0, S_LAND = 8, S_PRE = 16, S_BCNT = 20, S_ACNT = 24, S_PUB = 28, S_DONE1 = 32, S_AEND = 48, S_GO = 52, S_HOLD = 56, S_OBRDY = 60, S_GB2 = 64, S_XB = 68, S_VERD = 72 };
#define SHOFF 100.f
#define LMIN 9.8607613e-32f
#define BSLP 2
constexpr int QPITCH = 400;
constexpr unsigned CODE_LINES = 176;
constexpr int B1_X = 10240, B1_W = 24576, B1_ST = B1_X + B1_W;
constexpr int H_KVW = B1_ST, H_KVX = B1_ST + 16384;
static_assert(2 * B1_ST <= 81920 && H_KVX + 16384 <= 2 * B1_ST, "role B's region");
constexpr float QSCALE = 0.25f * 1.4426950408889634f;

#define STAMP(i) do { } while (0)
#define CSTAMP(i) do { } while (0)
#define STAMPDUMP() do { } while (0)

typedef __amdgpu_buffer_rsrc_t rsrc_t;

DI unsigned cvtpk(float lo, float hi) { f32x2_t v = {lo, hi}; bf16x2_t r = __builtin_convertvector(v, bf16x2_t); return __builtin_bit_cast(unsigned, r); }
DI bf16x8 as_frag(u32x4 v) { return __builtin_bit_cast(bf16x8, v); }
DI s16x4 vtr(const LAS char* p) { return __builtin_bit_cast(s16x4, __builtin_amdgcn_ds_read_tr16_b64_v4i16((LAS v4i16_t*)p)); }
DI bf16x8 trfrag(const LAS char* p0, const LAS char* p1) { s16x4 a = vtr(p0), b = vtr(p1); return __builtin_shufflevector(a, b, 0, 1, 2, 3, 4, 5, 6, 7); }
DI u32x4 ldg16(const void* p) { return *(const GAS u32x4*)p; }
DI float dot2bf(unsigned a, unsigned b, float c) { return __builtin_amdgcn_fdot2_f32_bf16(__builtin_bit_cast(bf16x2_t, a), __builtin_bit_cast(bf16x2_t, b), c, false); }
DI float swapsum(float x) { auto r = __builtin_amdgcn_permlane32_swap(__float_as_uint(x), __float_as_uint(x), false, false); return __uint_as_float(r[0]) + __uint_as_float(r[1]); }
DI float swapmax(float x) { auto r = __builtin_amdgcn_permlane32_swap(__float_as_uint(x), __float_as_uint(x), false, false); return fmaxf(__uint_as_float(r[0]), __uint_as_float(r[1])); }
DI void dma16(unsigned voff, const void* base, unsigned lds_base) {
    unsigned keep;
    asm volatile("s_mov_b32 %0, m0\n\ts_mov_b32 m0, %3\n\ts_nop 0\n\tglobal_load_lds_dwordx4 %1, %2\n\ts_mov_b32 m0, %0"
                 : "=&s"(keep) : "v"(voff), "s"(base), "s"(lds_base) : "memory");
}
DI void dma16_sc1(unsigned voff, const void* base, unsigned lds_base) {
    unsigned keep;
    asm volatile("s_mov_b32 %0, m0\n\ts_mov_b32 m0, %3\n\ts_nop 0\n\tglobal_load_lds_dwordx4 %1, %2 sc1\n\ts_mov_b32 m0, %0"
                 : "=&s"(keep) : "v"(voff), "s"(base), "s"(lds_base) : "memory");
}
DI void dma4(unsigned voff, const void* base, unsigned lds_base) {
    unsigned keep;
    asm volatile("s_mov_b32 %0, m0\n\ts_mov_b32 m0, %3\n\ts_nop 0\n\tglobal_load_lds_dwordx4 %1, %2\n\tglobal_load_lds_dwordx4 %1, %2 offset:1024\n\t"
                 "global_load_lds_dwordx4 %1, %2 offset:2048\n\tglobal_load_lds_dwordx4 %1, %2 offset:3072\n\ts_mov_b32 m0, %0"
                 : "=&s"(keep) : "v"(voff), "s"(base), "s"(lds_base) : "memory");
}
DI void dma4_sc1(unsigned voff, const void* base, unsigned lds_base) {
    unsigned keep;
    asm volatile("s_mov_b32 %0, m0\n\ts_mov_b32 m0, %3\n\ts_nop 0\n\tglobal_load_lds_dwordx4 %1, %2 sc1\n\tglobal_load_lds_dwordx4 %1, %2 offset:1024 sc1\n\t"
                 "global_load_lds_dwordx4 %1, %2 offset:2048 sc1\n\tglobal_load_lds_dwordx4 %1, %2 offset:3072 sc1\n\ts_mov_b32 m0, %0"
                 : "=&s"(keep) : "v"(voff), "s"(base), "s"(lds_base) : "memory");
}
#define PACE 1
#define PACEB() do { __builtin_amdgcn_s_sleep(PACE); SBAR(); } while (0)
DI void dmag1(unsigned v0, const void* base, unsigned lds_base) {
    asm volatile("s_mov_b32 m0, %2\n\ts_nop 0\n\tglobal_load_lds_dwordx4 %0, %1" :: "v"(v0), "s"(base), "s"(lds_base) : "memory");
}
DI void dmag2(unsigned v0, unsigned v1, const void* base, unsigned lds_base) {
    asm volatile("s_mov_b32 m0, %3\n\ts_nop 0\n\tglobal_load_lds_dwordx4 %0, %2\n\tglobal_load_lds_dwordx4 %1, %2 offset:1024" :: "v"(v0), "v"(v1), "s"(base), "s"(lds_base) : "memory");
}
DI void dmag4(unsigned v0, unsigned v1, unsigned v2, unsigned v3, const void* base, unsigned lds_base) {
    asm volatile("s_mov_b32 m0, %5\n\ts_nop 0\n\tglobal_load_lds_dwordx4 %0, %4\n\tglobal_load_lds_dwordx4 %1, %4 offset:1024\n\t"
                 "global_load_lds_dwordx4 %2, %4 offset:2048\n\tglobal_load_lds_dwordx4 %3, %4 offset:3072"
                 :: "v"(v0), "v"(v1), "v"(v2), "v"(v3), "s"(base), "s"(lds_base) : "memory");
}
DI void l2_touch(unsigned voff, const void* base, unsigned lds_corner) {
    asm volatile("s_mov_b32 m0, %2\n\ts_nop 0\n\tglobal_load_lds_dword %0, %1" :: "v"(voff), "s"(base), "s"(lds_corner) : "memory");
}
DI unsigned rfl(unsigned x) { return __builtin_amdgcn_readfirstlane(x); }
template <int OFF> DI void sig(unsigned a) { asm volatile("s_mov_b64 exec, 1\n\tds_add_u32 %0, %1 offset:%c2\n\ts_mov_b64 exec, -1" :: "v"(a), "v"(1u), "i"(OFF) : "memory"); }
template <int OFF> DI unsigned sig_rtn(unsigned a, unsigned val) {
    unsigned old;
    asm volatile("s_mov_b64 exec, 1\n\tds_add_rtn_u32 %0, %1, %2 offset:%c3\n\ts_waitcnt lgkmcnt(0)\n\ts_mov_b64 exec, -1" : "=&v"(old) : "v"(a), "v"(val), "i"(OFF) : "memory");
    return rfl(old);
}
template <int OFF> DI unsigned peek(unsigned a) { unsigned v; asm volatile("ds_read_b32 %0, %1 offset:%c2\n\ts_waitcnt lgkmcnt(0)" : "=&v"(v) : "v"(a), "i"(OFF) : "memory"); return rfl(v); }
template <int SLP, int OFF> DI void await(unsigned a, unsigned target) {
    unsigned v, sv, cnt;
    asm volatile("s_mov_b32 %2, 0x100000\n"
                 "1:\n\tds_read_b32 %0, %3 offset:%c6\n\ts_waitcnt lgkmcnt(0)\n\tv_readfirstlane_b32 %1, %0\n\ts_nop 0\n\ts_cmp_ge_u32 %1, %4\n\ts_cbranch_scc1 2f\n\t"
                 "s_sleep %5\n\ts_sub_u32 %2, %2, 1\n\ts_cbranch_scc0 1b\n"
                 "2:"
                 : "=&v"(v), "=&s"(sv), "=&s"(cnt) : "v"(a), "s"(rfl(target)), "i"(SLP), "i"(OFF) : "memory", "scc");
}
template <int OFF, int SLP = 0> DI void gbar(unsigned a, unsigned& gen, unsigned n) {
    asm volatile("s_waitcnt lgkmcnt(0)" ::: "memory");
    gen += n;
    sig<OFF>(a);
    await<SLP, OFF>(a, gen);
}

DI bool timed_out(rsrc_t rs, unsigned long long t0) {
    if (__builtin_amdgcn_s_memrealtime() - t0 > 2000000ull) {
        if ((threadIdx.x & 63) == 0) __builtin_amdgcn_raw_buffer_store_b32(1u, rs, WS_TMO, 0, SC1);
        return true;
    }
    return false;
}
DI void poll_flags(rsrc_t rs, unsigned off, bool act, unsigned tag) {
    const unsigned long long t0 = __builtin_amdgcn_s_memrealtime();
    for (;;) {
        unsigned v = tag;
        if (act) v = __builtin_amdgcn_raw_buffer_load_b32(rs, off, 0, SC1);
        if (__all(v == tag)) break;
        __builtin_amdgcn_s_sleep(POLLSLEEP);
        if (timed_out(rs, t0)) break;
    }
    asm volatile("" ::: "memory");
}
DI void poll_flags4(rsrc_t rs, unsigned off, unsigned tag) {
    const unsigned long long t0 = __builtin_amdgcn_s_memrealtime();
    for (;;) {
        const u32x4 v = __builtin_amdgcn_raw_buffer_load_b128(rs, off, 0, SC1);
        if (__all(v[0] == tag && v[1] == tag && v[2] == tag && v[3] == tag)) break;
        __builtin_amdgcn_s_sleep(POLLSLEEP);
        if (timed_out(rs, t0)) break;
    }
    asm volatile("" ::: "memory");
}

template <int NW>
DI void stage_w64(const u16* W, int col0, unsigned dst, int wave, int lane) {
    constexpr int NP = 64 / NW;
    const int rl = lane >> 3;
    const int ch0 = (lane & 7) ^ (((rl >> 1) & 1) << 2);
    const unsigned v0 = (unsigned)(((wave * NP * 8 + rl) * 512 + col0 + ch0 * 8) * 2);
#pragma unroll
    for (int i = 0; i < NP; i += 4) dmag4(v0, v0 + 7168, v0 + 14336, v0 + 21504, (const char*)W + i * 8192, rfl(dst + (wave * NP + i) * 1024));
}

DI void issue_chunk(const char* kvg, int c, unsigned dst, int i4, int lane) {
    const char* src = kvg + c * SLOT;
    dma4_sc1(lane * 16, src + i4 * 4096, rfl(dst + i4 * 4096));
    if (i4 == 0) dma16_sc1(lane * 16, src + 16384, rfl(dst + 16384));
}
DI void issue_xrow(const u16* xt, int row, unsigned dst, int lane) {
    dma16((unsigned)((lane ^ (row & 15)) << 4), (const char*)xt + row * 1024, rfl(dst + row * 1024));
}

DI float tilemax(const f32x16& s) {
    float m = fmaxf(fmaxf(s[0], s[1]), s[2]);
#pragma unroll
    for (int i = 3; i < 15; i += 2) m = fmaxf(fmaxf(m, s[i]), s[i + 1]);
    return fmaxf(m, s[15]);
}
DI void role_a(const u16* xA, const u16* wq, const u16* xB, const u16* wqkv, char* ws, rsrc_t rs, LAS char* lds, int b, int rb, int hg, int wave, unsigned& tag_out) {
    const int lane = threadIdx.x & 63;
    const int r = lane & 31, h = lane >> 5;
    const int qp = wave & 3, kh = wave >> 2;
    const unsigned lds0 = (unsigned)(uintptr_t)lds;
    const unsigned sy = lds0 + SYNC;
    const unsigned syk = sy + 4 * kh;
    const char* kvg = ws + WS_KV;
    unsigned tagv;
    STAMP(0);
    if (wave == 0 && lane < 32) *(LAS unsigned*)(lds + SYNC + lane * 4) = 0u;
    LDSBAR();
    {
        const u16* xt = xA + (size_t)rb * 64 * 512;
        const int kv0 = (hg >> 1) * 16;
#pragma unroll
        for (int i = 0; i < 6; ++i) { const int row = wave * 6 + i; issue_xrow(xt, row < kv0 ? row : row + 16, lds0 + RB, lane); }
        stage_w64<8>(wq, hg * 64, lds0, wave, lane);
        tagv = __builtin_amdgcn_raw_buffer_load_b32(rs, WS_EPOCH, 0, SC1);
        SBAR();
        DRAIN_VM();
        RAWBAR();
        STAMP(1);
        if (wave < 4) {
            const int hp = wave >> 1, ph = wave & 1;
            const int blk = (lane >> 4) & 1, q = (lane & 15) >> 2, p = lane & 3;
            const LAS char* pa = lds + (8 * h + q) * 128 + (((hp * 4 + blk * 2 + (p >> 1)) ^ (((q >> 1) & 1) << 2)) << 4) + (p & 1) * 8;
            const LAS char* xrow = lds + RB + (ph * 32 + r) * 1024;
            const LAS char* xp[8];
#pragma unroll
            for (int i = 0; i < 8; ++i) xp[i] = xrow + (((2 * i + h) ^ (r & 15)) << 4);
            f32x16 acc = {0};
            constexpr int PD = 4;
            bf16x8 xf[PD + 1], wf[PD + 1];
#pragma unroll
            for (int s = 0; s < PD; ++s) { xf[s] = *(const LAS bf16x8*)(xp[s & 7] + (s >> 3) * 256); wf[s] = trfrag(pa + s * 2048, pa + s * 2048 + 512); }
            SBAR();
#pragma unroll
            for (int s = 0; s < 32; ++s) {
                if (s + PD < 32) { xf[(s + PD) % (PD + 1)] = *(const LAS bf16x8*)(xp[(s + PD) & 7] + ((s + PD) >> 3) * 256); wf[(s + PD) % (PD + 1)] = trfrag(pa + (s + PD) * 2048, pa + (s + PD) * 2048 + 512); }
                SBAR();
                acc = MFMA32(wf[s % (PD + 1)], xf[s % (PD + 1)], acc);
                SBAR();
            }
            LAS char* dst = lds + QIMG + (hp * 128 + ph * 32 + r) * 32 + h * 16;
            *(LAS u32x4*)(dst) = (u32x4){cvtpk(acc[0] * QSCALE, acc[1] * QSCALE), cvtpk(acc[2] * QSCALE, acc[3] * QSCALE), cvtpk(acc[4] * QSCALE, acc[5] * QSCALE), cvtpk(acc[6] * QSCALE, acc[7] * QSCALE)};
            *(LAS u32x4*)(dst + 64 * 32) = (u32x4){cvtpk(acc[8] * QSCALE, acc[9] * QSCALE), cvtpk(acc[10] * QSCALE, acc[11] * QSCALE), cvtpk(acc[12] * QSCALE, acc[13] * QSCALE), cvtpk(acc[14] * QSCALE, acc[15] * QSCALE)};
        }
        STAMP(2);
        LDSBAR();
        STAMP(3);
    }
    const unsigned tag = rfl(tagv) + 1u;
    const unsigned sA = (unsigned)(2 * kh) * SLOT, sB = sA + SLOT;
    unsigned agen = 0;
    issue_chunk(kvg, 4 * kh, lds0 + sA, qp, lane);
    bf16x8 qfa = *(const LAS bf16x8*)(lds + QIMG + (qp * 64 + r) * 32 + h * 16);
    bf16x8 qfb = *(const LAS bf16x8*)(lds + QIMG + (qp * 64 + 32 + r) * 32 + h * 16);
    DRAIN_VM();
    RAWBAR();
    const int kr = h * 512 + r * 16, vr = 8192 + h * VBLK + (r < 17 ? r : 17) * 16;
    f32x16 oa, ob;
    float sha = 0.f, shb = 0.f;
    for (int attempt = 0;; ++attempt) {
        STAMP(4); CSTAMP(12);
        bf16x8 kc = *(const LAS bf16x8*)(lds + sA + kr);
        bf16x8 kn = *(const LAS bf16x8*)(lds + sA + kr + 1024);
        bf16x8 v0 = *(const LAS bf16x8*)(lds + sA + vr);
        bf16x8 v1 = *(const LAS bf16x8*)(lds + sA + vr + 2 * VBLK);
        f32x16 cia, cib, sc;
        if (attempt == 0) {
            const bf16x8 k0 = *(const LAS bf16x8*)(lds + kr);
            const f32x16 z = {0};
            sha = swapmax(tilemax(MFMA32(k0, qfa, z))) + SHOFF;
            shb = swapmax(tilemax(MFMA32(k0, qfb, z))) + SHOFF;
        }
#pragma unroll
        for (int i = 0; i < 16; ++i) { cia[i] = -sha; cib[i] = -shb; }
        sc = MFMA32(kc, qfa, cia);
        oa = (f32x16){0}; ob = (f32x16){0};
        bf16x8 v1p = v1; u32x4 pp = {0u, 0u, 0u, 0u};
        unsigned s_cur = sA, s_nxt = sB;
#define EX(i) __builtin_amdgcn_exp2f(sc[i])
#define VTILE(PEND, NEXT, MINE, MID) do { \
            f32x16 sn; u32x4 p0, p1; \
            float e0 = EX(0), e1 = EX(1); PINV(e0); PINV(e1); SBAR(); \
            PEND; SBAR(); \
            float e2 = EX(2), e3 = EX(3); p0[0] = cvtpk(e0, e1); float e4 = EX(4), e5 = EX(5); p0[1] = cvtpk(e2, e3); float e6 = EX(6); \
            PINV(p0); PINV(e4); PINV(e5); PINV(e6); SBAR(); \
            sn = NEXT; PINV(sn); SBAR(); \
            float e7 = EX(7); p0[2] = cvtpk(e4, e5); float e8 = EX(8); p0[3] = cvtpk(e6, e7); PINV(p0); PINV(e8); SBAR(); \
            float e9 = EX(9), e10 = EX(10), e11 = EX(11); PINV(e9); PINV(e10); PINV(e11); SBAR(); \
            MINE; SBAR(); \
            MID; \
            float e12 = EX(12); p1[0] = cvtpk(e8, e9); float e13 = EX(13); p1[1] = cvtpk(e10, e11); float e14 = EX(14), e15 = EX(15); \
            p1[2] = cvtpk(e12, e13); p1[3] = cvtpk(e14, e15); PINV(p1); SBAR(); \
            pp = p1; sc = sn; } while (0)
        for (int j = 0; j < 4; ++j) {
            const LAS char* kcp = lds + s_cur + kr; const LAS char* vcp = lds + s_cur + vr;
            const LAS char* kxp = lds + s_nxt + kr; const LAS char* vxp = lds + s_nxt + vr;
#pragma unroll
            for (int t = 0; t < 8; ++t) {
                bf16x8 k2, nv0, nv1;
                if (t == 0) __builtin_amdgcn_s_setprio(2); else if (t == 3) __builtin_amdgcn_s_setprio(1); else if (t == 6) __builtin_amdgcn_s_setprio(0);
                VTILE({ ob = MFMA32(v1p, as_frag(pp), ob); PINV(ob); }, MFMA32(kc, qfb, cib), { oa = MFMA32(v0, as_frag(p0), oa); PINV(oa); },
                      { k2 = (t < 6) ? *(const LAS bf16x8*)(kcp + (t + 2) * 1024) : *(const LAS bf16x8*)(kxp + (t - 6) * 1024); SBAR();
                        nv1 = (t < 7) ? *(const LAS bf16x8*)(vcp + (t + 1) * 4 * VBLK + 2 * VBLK) : *(const LAS bf16x8*)(vxp + 2 * VBLK); SBAR();
                        nv0 = (t < 7) ? *(const LAS bf16x8*)(vcp + (t + 1) * 4 * VBLK) : *(const LAS bf16x8*)(vxp); SBAR(); });
                VTILE({ oa = MFMA32(v1, as_frag(pp), oa); PINV(oa); }, MFMA32(kn, qfa, cia), { ob = MFMA32(v0, as_frag(p0), ob); PINV(ob); }, { });
                v1p = v1; v1 = nv1; v0 = nv0; kc = kn; kn = k2;
                if (t == 0) {
                    if (j >= 1) sig<S_CONS>(syk);
                    else if (attempt == 0) issue_chunk(kvg, 4 * kh + 1, lds0 + s_nxt, qp, lane);
                    SBAR();
                }
                if (t == 1) {
                    if (j == 1 || j == 2) {
                        await<0, S_CONS>(syk, 12 * attempt + 4 * j);
                        issue_chunk(kvg, 4 * kh + j + 1, lds0 + s_nxt, qp, lane);
                    }
                    SBAR();
                }
                if (t == 4) { if (j < 3) { DRAIN_VM(); sig<S_LAND>(syk); } SBAR(); }
                if (t == 5) {
                    if (j < 3) await<0, S_LAND>(syk, 12 * attempt + 4 * (j + 1));
                    SBAR();
                }
            }
            const unsigned tmp = s_cur; s_cur = s_nxt; s_nxt = tmp;
        }
        ob = MFMA32(v1p, as_frag(pp), ob);
#undef EX
#undef VTILE
        __builtin_amdgcn_s_setprio(3);
        sig<S_AEND>(sy);
        STAMP(9);
        int lane2 = lane; PINV(lane2);
        const int r2 = lane2 & 31, h2 = lane2 >> 5;
        LAS char* part = lds + 2 * SLOT + qp * 4352;
        if (kh == 1) {
            await<0, S_CONS>(syk, 12 * attempt + 12);
            *(LAS f32x4*)(part + lane2 * 16) = (f32x4){oa[0], oa[1], oa[2], oa[3]};
            *(LAS f32x4*)(part + 1024 + lane2 * 16) = (f32x4){oa[4], oa[5], oa[6], oa[7]};
            *(LAS f32x4*)(part + 2176 + lane2 * 16) = (f32x4){ob[0], ob[1], ob[2], ob[3]};
            *(LAS f32x4*)(part + 2176 + 1024 + lane2 * 16) = (f32x4){ob[4], ob[5], ob[6], ob[7]};
            if (h2 == 0) { *(LAS float*)(part + 2048 + r2 * 4) = oa[8]; *(LAS float*)(part + 2176 + 2048 + r2 * 4) = ob[8]; }
            sig<S_DONE1>(sy + 4 * qp);
        } else {
            await<1, S_DONE1>(sy + 4 * qp, attempt + 1);
            const f32x4 a0 = *(const LAS f32x4*)(part + lane2 * 16), a1 = *(const LAS f32x4*)(part + 1024 + lane2 * 16);
            const f32x4 b0 = *(const LAS f32x4*)(part + 2176 + lane2 * 16), b1 = *(const LAS f32x4*)(part + 2176 + 1024 + lane2 * 16);
            const float la = oa[8] + *(const LAS float*)(part + 2048 + r2 * 4), lb = ob[8] + *(const LAS float*)(part + 2176 + 2048 + r2 * 4);
            const float sa = swapsum(h2 == 0 ? la : 0.f), sb_ = swapsum(h2 == 0 ? lb : 0.f);
            const float ra = __builtin_amdgcn_rcpf(sa), rb_ = __builtin_amdgcn_rcpf(sb_);
            float amax = 0.f;
#pragma unroll
            for (int i = 0; i < 4; ++i) {
                oa[i] += a0[i]; oa[4 + i] += a1[i]; ob[i] += b0[i]; ob[4 + i] += b1[i];
            }
            const unsigned off = WS_OA + (unsigned)((rb * 32 + hg * 4 + qp) * 64 + r2) * 32u + h2 * 16u;
            __builtin_amdgcn_raw_buffer_store_b128((u32x4){cvtpk(oa[0] * ra, oa[1] * ra), cvtpk(oa[2] * ra, oa[3] * ra), cvtpk(oa[4] * ra, oa[5] * ra), cvtpk(oa[6] * ra, oa[7] * ra)}, rs, off, 0, SC1);
            __builtin_amdgcn_raw_buffer_store_b128((u32x4){cvtpk(ob[0] * rb_, ob[1] * rb_), cvtpk(ob[2] * rb_, ob[3] * rb_), cvtpk(ob[4] * rb_, ob[5] * rb_), cvtpk(ob[6] * rb_, ob[7] * rb_)}, rs, off + 1024u, 0, SC1);
            SBAR();
            bool ok = (sa >= LMIN) && (sa <= 1.2676506e30f) && (sb_ >= LMIN) && (sb_ <= 1.2676506e30f);
#pragma unroll
            for (int i = 0; i < 8; ++i) ok = ok && (fabsf(oa[i]) <= 3.0e38f) && (fabsf(ob[i]) <= 3.0e38f);
            (void)amax;
            const bool bad = attempt == 0 && !__all(ok);
            (void)sig_rtn<S_VERD>(sy, bad ? 0x10001u : 1u);
            DRAIN_VM();
            const unsigned tot = sig_rtn<S_PUB>(sy, 1u) + 1u;
            if (tot == 4u * (attempt + 1)) {
                unsigned vd;
                for (unsigned spins = 0; ((vd = peek<S_VERD>(sy)) & 0xffffu) < 4u * (attempt + 1) && spins < (1u << 18); ++spins) { }
                if (attempt == 1 || (vd >> 16) == 0) {
                    unsigned tg = tag; PINV(tg);
                    if (lane2 == 0) __builtin_amdgcn_raw_buffer_store_b32(tg, rs, WS_FLAG_A + (rb * 8 + hg) * FSTR, 0, SC1);
                }
            }
        }
        STAMP(5); CSTAMP(13);
        unsigned pub;
        for (unsigned spins = 0; ((pub = peek<S_VERD>(sy)) & 0xffffu) < 4u * (attempt + 1) && spins < (1u << 18); ++spins) __builtin_amdgcn_s_sleep(1);
        if (attempt == 1 || (pub >> 16) == 0) break;
        float ma = -INFINITY, mb = -INFINITY;
        for (int t = 0; t < 64; ++t) {
            const u32x4 kf = __builtin_amdgcn_raw_buffer_load_b128(rs, WS_KV + (t >> 3) * SLOT + (t & 7) * 1024 + kr, 0, SC1);
            const f32x16 z = {0};
            ma = fmaxf(ma, tilemax(MFMA32(as_frag(kf), qfa, z)));
            mb = fmaxf(mb, tilemax(MFMA32(as_frag(kf), qfb, z)));
        }
        sha = swapmax(ma); shb = swapmax(mb);
        gbar<S_ACNT>(sy, agen, 8);
        issue_chunk(kvg, 4 * kh, lds0 + sA, qp, lane); issue_chunk(kvg, 4 * kh + 1, lds0 + sB, qp, lane);
        DRAIN_VM();
        gbar<S_ACNT>(sy, agen, 8);
    }
    tag_out = tag;
    if (kh == 0) STAMPDUMP();
    if (b == 0 && wave == 0) {
        const unsigned long long t0 = __builtin_amdgcn_s_memrealtime();
        for (;;) {
            unsigned v[8];
#pragma unroll
            for (int i = 0; i < 8; ++i) v[i] = __builtin_amdgcn_raw_buffer_load_b32(rs, WS_FLAG_A + (i * 64 + lane) * FSTR, 0, SC1);
            bool all = true;
#pragma unroll
            for (int i = 0; i < 8; ++i) all = all && v[i] == tag;
            if (__all(all) || timed_out(rs, t0)) break;
            __builtin_amdgcn_s_sleep(2);
        }
        if (lane == 0) __builtin_amdgcn_raw_buffer_store_b32(tag, rs, WS_EPOCH, 0, SC1);
    }
}

DI void role_b(const u16* xA, const u16* wq, const u16* xB, const u16* wqkv, const u16* wk, const u16* wv, const u16* woA, const u16* woB, u16* out,
               char* ws, rsrc_t rs, LAS char* ldsall, int b, int rb, int hg, int wave, unsigned& tag_out, unsigned& gen_out) {
    const int lane = threadIdx.x & 63;
    const int r = lane & 31, h = lane >> 5;
    const int g = lane >> 4, c16 = lane & 15;
    LAS char* lds = ldsall + RB;
    const unsigned lds0 = (unsigned)(uintptr_t)lds;
    const unsigned sy = (unsigned)(uintptr_t)ldsall + SYNC;
    unsigned tag = 0, tagv, gen = 0;
    __builtin_amdgcn_s_setprio(BPRIO);
    STAMP(0);
    const int wch = (lane & 7) ^ (((lane >> 4) & 1) << 2);
    const unsigned av0 = (unsigned)(((wave & 1) * 32 + (lane >> 3)) * 3072 + (wave >> 1) * 1024 + hg * 128 + wch * 16);
    unsigned bv[4], cv;
    {
        const int che = (lane & 7) ^ ((lane >> 4) & 7), cho = (lane & 7) ^ ((4 + (lane >> 4)) & 7);
#pragma unroll
        for (int i = 0; i < 4; ++i) {
            int gr = rb * 64 - 16 + ((wave & 1) * 4 + i) * 8 + (lane >> 3); gr = gr < 0 ? 0 : gr;
            const unsigned xo = (unsigned)(gr * 1024 + ((i & 1) ? cho : che) * 16 - i * 1024 + 4096);
            const unsigned wo = (unsigned)(((wave & 1) * 32 + i * 8 + (lane >> 3)) * 3072 + 2048 + hg * 128 + wch * 16 - i * 1024);
            bv[i] = wave < 2 ? wo : xo;
        }
        cv = wave < 2 ? (unsigned)((rb * 64 - 16 + (8 + wave) * 8 + (lane >> 3)) * 1024 + (wave ? cho : che) * 16 + 4096) : bv[0];
    }
    const char* srcA = (const char*)wqkv;
    const char* srcB = wave < 2 ? (const char*)wqkv : (const char*)xB - 4096;
    const char* srcC = (const char*)xB - 4096;
    const unsigned advB = wave < 2 ? 64 * 3072 : 128;
    const unsigned dA = B1_X + wave * 4096, dB = wave < 2 ? B1_X + 16384 + wave * 4096 : (wave - 2) * 4096, dC = wave < 2 ? (8 + wave) * 1024 : dB;
    int pfr = rb * 64 - 16 + lane; pfr = pfr < 0 ? 0 : pfr;
    const unsigned pfo = wave < 3 ? (unsigned)(lane * 3072 + wave * 1024 + hg * 128) : (unsigned)(pfr * 1024);
    const unsigned pfo2 = (unsigned)((rb * 64 + 48 + (lane & 15)) * 1024);
    auto touch_step = [&](int k) {
    };
    auto issue_step = [&](unsigned buf) {
        dmag4(av0, av0 + 23552, av0 + 47104, av0 + 70656, srcA, rfl(lds0 + buf + dA));
        dmag4(bv[0], bv[1], bv[2], bv[3], srcB, rfl(lds0 + buf + dB));
        dmag1(cv, srcC, rfl(lds0 + buf + dC));
        srcA += 64 * 3072; srcB += advB; srcC += 128;
    };
    {
        const int which = hg & 1, kv0 = (hg >> 1) * 16, t16 = rb * 4 + (hg >> 1);
        const u16* xt = xA + (size_t)rb * 64 * 512;
        {
            const u16* W = which ? wv : wk;
            dma4(lane * 16, (const char*)W + wave * 4096, rfl(lds0 + XCH + wave * 4096));
#pragma unroll
            for (int i = 0; i < 4; ++i) issue_xrow(xt, kv0 + wave * 4 + i, lds0, lane);
            tagv = __builtin_amdgcn_raw_buffer_load_b32(rs, WS_EPOCH, 0, SC1);
            SBAR();
        }
        RAWBAR();
        DRAIN_VM();
        gbar<S_BCNT>(sy, gen, 4);
        tag = rfl(tagv) + 1u;
        if (wave == 0) {
            STAMP(7);
            const LAS char* base = lds + XCH + (8 * g + (c16 >> 2)) * 32 + (lane & 3) * 8;
            const LAS char* xrow = lds + (kv0 + c16) * 1024;
            f32x4 acc = {0, 0, 0, 0}, acc2 = {0, 0, 0, 0};
#pragma unroll 1
            for (int s4 = 0; s4 < 2; ++s4) {
                bf16x8 xf[8], wf[8];
#pragma unroll
                for (int s = 0; s < 8; ++s) { xf[s] = *(const LAS bf16x8*)(xrow + (((4 * (s & 3) + g) ^ c16) << 4) + (s4 * 2 + (s >> 2)) * 256); wf[s] = trfrag(base + (s4 * 8 + s) * 1024, base + (s4 * 8 + s) * 1024 + 128); }
                SBAR();
                if (which == 0) {
#pragma unroll
                    for (int s = 0; s < 8; s += 2) { acc = MFMA16(wf[s], xf[s], acc); acc2 = MFMA16(wf[s + 1], xf[s + 1], acc2); }
                } else {
#pragma unroll
                    for (int s = 0; s < 8; s += 2) { acc = MFMA16(xf[s], wf[s], acc); acc2 = MFMA16(xf[s + 1], wf[s + 1], acc2); }
                }
            }
            acc += acc2;
            const u32x2 w = {cvtpk(acc[0], acc[1]), cvtpk(acc[2], acc[3])};
            const int t32 = t16 >> 1, s16 = t16 & 1, t = t32 & 7;
            const unsigned cbase = WS_KV + (t32 >> 3) * SLOT;
            const unsigned vb = cbase + 8192 + (t * 2 + s16) * 2 * VBLK;
            const unsigned off = which ? vb + (g & 1) * VBLK + c16 * 16 + (g >> 1) * 8 : cbase + t * 1024 + (g & 1) * 512 + (s16 * 16 + c16) * 16 + (g >> 1) * 8;
            __builtin_amdgcn_raw_buffer_store_b64(w, rs, off, 0, SC1);
            if (which && lane < 8) {
                const unsigned val = (lane & 2) ? 0u : 0x3f803f80u;
                __builtin_amdgcn_raw_buffer_store_b64((u32x2){val, val}, rs, vb + (lane >> 2) * VBLK + 256 + (lane & 3) * 8, 0, SC1);
            }
            DRAIN_VM();
            if (lane == 0) __builtin_amdgcn_raw_buffer_store_b32(tag, rs, WS_FLAG_KV + (rb * 8 + hg) * FSTR, 0, SC1);
            STAMP(8);
        }
        RAWBAR();
        poll_flags(rs, WS_FLAG_KV + (wave * 64 + lane) * FSTR, true, tag);
        STAMP(9);
        RAWBAR();
        RAWBAR();
        issue_step(0);
    }
    {
        const int rtA = (wave == 0) ? 0 : (wave == 3 ? 2 : 1), rtB = (wave == 0) ? 0 : (wave == 1 ? 1 : 2);
        const int sgA = (wave == 0) ? 1 : (wave == 2 ? 2 : 0), sgB = (wave == 0) ? 2 : (wave == 2 ? 2 : 1);
        int irA = rtA * 32 - 16 + r; irA = irA < 0 ? 0 : irA;
        int irB = rtB * 32 - 16 + r; irB = irB < 0 ? 0 : irB;
        int xa[4], xb_[4];
#pragma unroll
        for (int i = 0; i < 4; ++i) { xa[i] = irA * 128 + (((2 * i + h) ^ ((irA >> 1) & 7)) << 4); xb_[i] = irB * 128 + (((2 * i + h) ^ ((irB >> 1) & 7)) << 4); }
        const int blk = (lane >> 4) & 1, q = (lane & 15) >> 2, p = lane & 3;
        const int ch0 = (blk * 2 + (p >> 1)) ^ (((q >> 1) & 1) << 2);
        const int rdl = B1_X + (8 * h + q) * 128 + (p & 1) * 8;
        const int a0 = rdl + sgA * 8192 + ch0 * 16, a1 = rdl + sgA * 8192 + (ch0 ^ 4) * 16;
        const int b0 = rdl + sgB * 8192 + ch0 * 16, b1 = rdl + sgB * 8192 + (ch0 ^ 4) * 16;
        f32x16 acc0 = {0}, acc1 = {0}, acc2 = {0}, acc3 = {0};
        unsigned buf = 0;
#pragma unroll 1
        for (int k = 0; k < 8; ++k) {
            DRAIN_VM();
            gbar<S_BCNT, BSLP>(sy, gen, 4);
            const bool paced = true;
            if (k < 7) issue_step(B1_ST - buf);
            if (k < 6) touch_step(k + 2);
            if (k == 5) {
                l2_touch((unsigned)((wave * 128 + lane) * 1024 + hg * 128), woB, sy + 640); l2_touch((unsigned)((wave * 128 + 64 + lane) * 1024 + hg * 128), woB, sy + 640);
            }
            const LAS char* sb = lds + buf;
#pragma unroll
            for (int hs = 0; hs < 2; ++hs) {
                bf16x8 fx[2][2], fw[2][4];
#pragma unroll
                for (int s = 0; s < 2; ++s) {
                    const int ks = 2 * hs + s;
                    fx[s][0] = *(const LAS bf16x8*)(sb + xa[ks]);
                    fw[s][0] = trfrag(sb + a0 + ks * 2048, sb + a0 + ks * 2048 + 512); fw[s][1] = trfrag(sb + a1 + ks * 2048, sb + a1 + ks * 2048 + 512);
                    fx[s][1] = *(const LAS bf16x8*)(sb + xb_[ks]);
                    fw[s][2] = trfrag(sb + b0 + ks * 2048, sb + b0 + ks * 2048 + 512); fw[s][3] = trfrag(sb + b1 + ks * 2048, sb + b1 + ks * 2048 + 512);
                }
                SBAR();
#define BSTEP(ACC, FW, FX) do { ACC = MFMA32(FW, FX, ACC); PINV(ACC); SBAR(); if (paced) __builtin_amdgcn_s_sleep(PACE); SBAR(); } while (0)
                BSTEP(acc0, fw[0][0], fx[0][0]); BSTEP(acc1, fw[0][1], fx[0][0]); BSTEP(acc2, fw[0][2], fx[0][1]); BSTEP(acc3, fw[0][3], fx[0][1]);
                BSTEP(acc0, fw[1][0], fx[1][0]); BSTEP(acc1, fw[1][1], fx[1][0]); BSTEP(acc2, fw[1][2], fx[1][1]); BSTEP(acc3, fw[1][3], fx[1][1]);
#undef BSTEP
            }
            buf = B1_ST - buf;
        }
        gbar<S_BCNT>(sy, gen, 4);
        STAMP(2);
#define PK8(A, o) (u32x4){cvtpk(A[o], A[o + 1]), cvtpk(A[o + 2], A[o + 3]), cvtpk(A[o + 4], A[o + 5]), cvtpk(A[o + 6], A[o + 7])}
        if (rtA != 0 || r >= 16) {
            LAS char* qa = lds + (rtA * 32 - 16 + r) * QPITCH + sgA * 128 + h * 16;
            *(LAS u32x4*)(qa + 0) = PK8(acc0, 0); *(LAS u32x4*)(qa + 32) = PK8(acc0, 8); *(LAS u32x4*)(qa + 64) = PK8(acc1, 0); *(LAS u32x4*)(qa + 96) = PK8(acc1, 8);
            LAS char* qb = lds + (rtB * 32 - 16 + r) * QPITCH + sgB * 128 + h * 16;
            *(LAS u32x4*)(qb + 0) = PK8(acc2, 0); *(LAS u32x4*)(qb + 32) = PK8(acc2, 8); *(LAS u32x4*)(qb + 64) = PK8(acc3, 0); *(LAS u32x4*)(qb + 96) = PK8(acc3, 8);
        }
#undef PK8
        gbar<S_BCNT>(sy, gen, 4);
    }
#pragma unroll 1
    for (int ph = 0; ph < 2; ++ph) {
        const int pos = ph * 32 + r;
        const LAS char* row = lds + (16 + pos) * QPITCH + wave * 32;
        const u32x4 q0 = *(const LAS u32x4*)(row), q1 = *(const LAS u32x4*)(row + 16);
        const LAS char* krow = row + 128 - 8 * h * QPITCH;
        float sc[4];
#pragma unroll
        for (int i = 0; i < 4; ++i) {
            const u32x4 k0 = *(const LAS u32x4*)(krow - 2 * i * QPITCH), k1 = *(const LAS u32x4*)(krow - 2 * i * QPITCH + 16);
            float s = 0.f, s2 = 0.f;
#pragma unroll
            for (int j = 0; j < 4; ++j) { s = dot2bf(q0[j], k0[j], s); s2 = dot2bf(q1[j], k1[j], s2); }
            sc[i] = (rb * 64 + pos - 2 * (4 * h + i) >= 0) ? (s + s2) * QSCALE : -INFINITY;
        }
        const float m = swapmax(fmaxf(fmaxf(sc[0], sc[1]), fmaxf(sc[2], sc[3])));
        float pw[4];
#pragma unroll
        for (int i = 0; i < 4; ++i) pw[i] = __builtin_amdgcn_exp2f(sc[i] - m);
        const float l = swapsum((pw[0] + pw[1]) + (pw[2] + pw[3]));
        const unsigned pa = cvtpk(pw[0], pw[1]), pb = cvtpk(pw[2], pw[3]);
        const auto ra = __builtin_amdgcn_permlane32_swap(pa, pa, false, false);
        const auto rb2 = __builtin_amdgcn_permlane32_swap(pb, pb, false, false);
        const unsigned pp[4] = {ra[0], rb2[0], ra[1], rb2[1]};
        float o[8];
#pragma unroll
        for (int j = 0; j < 8; ++j) o[j] = 0.f;
        const LAS char* vrow = row + 256 + h * 16;
#pragma unroll
        for (int w2 = 0; w2 < 4; ++w2) {
            const u32x4 va = *(const LAS u32x4*)(vrow - 4 * w2 * QPITCH), vb = *(const LAS u32x4*)(vrow - (4 * w2 + 2) * QPITCH);
#pragma unroll
            for (int j = 0; j < 4; ++j) {
                o[2 * j] = dot2bf(__builtin_amdgcn_perm(vb[j], va[j], 0x05040100u), pp[w2], o[2 * j]);
                o[2 * j + 1] = dot2bf(__builtin_amdgcn_perm(vb[j], va[j], 0x07060302u), pp[w2], o[2 * j + 1]);
            }
        }
        const float rl = __builtin_amdgcn_rcpf(l);
        u32x4 ow = {cvtpk(o[0] * rl, o[1] * rl), cvtpk(o[2] * rl, o[3] * rl), cvtpk(o[4] * rl, o[5] * rl), cvtpk(o[6] * rl, o[7] * rl)};
        __builtin_amdgcn_raw_buffer_store_b128(ow, rs, WS_OB + (unsigned)((rb * 32 + hg * 4 + wave) * 64 + pos) * 32u + h * 16u, 0, SC1);
    }
    DRAIN_VM();
    gbar<S_BCNT>(sy, gen, 4);
    if (wave == 0 && lane == 0) __builtin_amdgcn_raw_buffer_store_b32(tag, rs, WS_FLAG_B + (rb * 8 + hg) * FSTR, 0, SC1);
    STAMP(3);
    stage_w64<4>(woB, hg * 64, lds0, wave, lane);
#pragma unroll
    for (int i = 0; i < 2; ++i)
        asm volatile("s_mov_b32 m0, %2\n\ts_nop 0\n\tglobal_load_lds_dword %0, %1" :: "v"((unsigned)(((wave * 2 + i) * 64 + lane) * 1024 + hg * 128)), "s"(woA), "s"(sy + 640) : "memory");
    DRAIN_VM();
    gbar<S_BCNT, 1>(sy, gen, 4);
    if (wave == 0) { poll_flags(rs, WS_FLAG_B + (rb * 8 + (lane & 7)) * FSTR, lane < 8, tag); sig<S_OBRDY>(sy); }
    STAMP(4);
    {
        unsigned pub;
        for (unsigned spins = 0; spins < (1u << 18); ++spins) {
            pub = peek<S_VERD>(sy);
            if ((pub & 0xffffu) >= 8u || ((pub & 0xffffu) == 4u && (pub >> 16) == 0)) break;
            __builtin_amdgcn_s_sleep(2);
        }
    }
    stage_w64<4>(woA, hg * 64, (unsigned)(uintptr_t)ldsall, wave, lane);
    DRAIN_VM();
    gbar<S_BCNT>(sy, gen, 4);
    STAMP(5);
    tag_out = tag; gen_out = gen;
}

constexpr int OPD = 6;
DI void out_tile(int br, int wave, unsigned tag, unsigned gen, u16* out, char* ws, rsrc_t rs, LAS char* ldsall, int rb, int hg) {
    const int lane = threadIdx.x & 63;
    const int r = lane & 31, h = lane >> 5;
    const unsigned sy = (unsigned)(uintptr_t)ldsall + SYNC;
    const int kh = wave & 1, ph = wave >> 1;
    const int blk = (lane >> 4) & 1, q = (lane & 15) >> 2, p = lane & 3;
    const int ch0 = (blk * 2 + (p >> 1)) ^ (((q >> 1) & 1) << 2);
    const LAS char* rowp = ldsall + (br ? RB : 0) + (kh * 256 + 4 * h + q) * 128 + (p & 1) * 8;
    const LAS char* b0 = rowp + ch0 * 16; const LAS char* b1 = rowp + (ch0 ^ 4) * 16;
    if (br) await<1, S_OBRDY>(sy, 1);
    bf16x8 w0[16], w1[16];
#pragma unroll
    for (int s = 0; s < OPD; ++s) { w0[s] = trfrag(b0 + s * 2048, b0 + s * 2048 + 1024); w1[s] = trfrag(b1 + s * 2048, b1 + s * 2048 + 1024); }
#pragma unroll
    for (int s = 0; s < OPD; ++s) { PINV(w0[s]); PINV(w1[s]); }
    if (!br) poll_flags(rs, WS_FLAG_A + (rb * 8 + (lane & 7)) * FSTR, lane < 8, tag);
    STAMP(6);
    u32x4 bfr[16];
    const unsigned obase = (br ? WS_OB : WS_OA) + (unsigned)((rb * 32 + kh * 16) * 64 + ph * 32 + r) * 32u + h * 16u;
#pragma unroll
    for (int s = 0; s < 16; ++s) bfr[s] = __builtin_amdgcn_raw_buffer_load_b128(rs, obase + s * 2048u, 0, SC1);
    SBAR();
    f32x16 acc0 = {0}, acc1 = {0};
#pragma unroll
    for (int s = 0; s < 16; ++s) {
        acc0 = MFMA32(w0[s], as_frag(bfr[s]), acc0);
        acc1 = MFMA32(w1[s], as_frag(bfr[s]), acc1);
        if (s + OPD < 16) { w0[s + OPD] = trfrag(b0 + (s + OPD) * 2048, b0 + (s + OPD) * 2048 + 1024); w1[s + OPD] = trfrag(b1 + (s + OPD) * 2048, b1 + (s + OPD) * 2048 + 1024); }
        SBAR();
    }
    LAS f32x4* mine = (LAS f32x4*)(ldsall + RB + XCH + wave * 4096);
    const LAS f32x4* theirs = (const LAS f32x4*)(ldsall + RB + XCH + (wave ^ 1) * 4096);
    const f32x16 give = kh ? acc0 : acc1, keep = kh ? acc1 : acc0;
    if (!br) await<0, S_XB>(sy, 4);
#pragma unroll
    for (int i = 0; i < 4; ++i) mine[i * 64 + lane] = (f32x4){give[4 * i], give[4 * i + 1], give[4 * i + 2], give[4 * i + 3]};
    gbar<0>(sy + (br ? S_GB2 : S_BCNT), gen, 4);
    f32x4 t[4];
#pragma unroll
    for (int i = 0; i < 4; ++i) t[i] = theirs[i * 64 + lane];
    if (br) { asm volatile("s_waitcnt lgkmcnt(0)" ::: "memory"); sig<S_XB>(sy); }
    unsigned w[8];
#pragma unroll
    for (int i = 0; i < 4; ++i) { w[2 * i] = cvtpk(keep[4 * i] + t[i][0], keep[4 * i + 1] + t[i][1]); w[2 * i + 1] = cvtpk(keep[4 * i + 2] + t[i][2], keep[4 * i + 3] + t[i][3]); }
    u16* dst = out + (size_t)(rb * 64 + ph * 32 + r) * 1024 + br * 512 + hg * 64 + kh * 32 + h * 8;
#pragma unroll
    for (int i = 0; i < 2; ++i) {
        auto r0 = __builtin_amdgcn_permlane32_swap(w[4 * i], w[4 * i + 2], false, false);
        auto r1 = __builtin_amdgcn_permlane32_swap(w[4 * i + 1], w[4 * i + 3], false, false);
        *(GAS u32x4*)(dst + i * 16) = (u32x4){r0[0], r1[0], r0[1], r1[1]};
    }
    STAMP(10);
    STAMPDUMP();
}

__global__ void __launch_bounds__(768, 3) fused_kernel(const u16* xA, const u16* wq, const u16* wk, const u16* wv, const u16* woA,
                                                       const u16* xB, const u16* wqkv, const u16* woB, u16* out, char* ws) {
    extern __shared__ __attribute__((aligned(16))) char smem[];
    LAS char* lds = (LAS char*)smem;
    const rsrc_t rs = __builtin_amdgcn_make_buffer_rsrc(ws, 0, WS_TOTAL, 0x00020000);
    const int b = blockIdx.x;
    const int rb = ((b & 7) >> 1) * 8 + ((b >> 3) & 7), hg = (b & 1) * 4 + (b >> 6);
    const int wave = __builtin_amdgcn_readfirstlane(threadIdx.x >> 6);
    unsigned tag = 0, gen = 0;
    if (wave < 8) role_a(xA, wq, xB, wqkv, ws, rs, lds, b, rb, hg, wave, tag);
    else role_b(xA, wq, xB, wqkv, wk, wv, woA, woB, out, ws, rs, lds, b, rb, hg, wave - 8, tag, gen);
    if (wave >= 4) out_tile(wave < 8 ? 1 : 0, wave & 3, tag, gen, out, ws, rs, lds, rb, hg);
}

extern "C" void kernel_launch(void* const* d_in, const int* in_sizes, int n_in, void* d_out, int out_size, void* d_ws, size_t ws_size,
                              hipStream_t stream) {
    (void)in_sizes; (void)n_in; (void)out_size; (void)ws_size;
    (void)hipFuncSetAttribute((const void*)fused_kernel, hipFuncAttributeMaxDynamicSharedMemorySize, LDS_BYTES);
    fused_kernel<<<256, 768, LDS_BYTES, stream>>>((const u16*)d_in[0], (const u16*)d_in[1], (const u16*)d_in[2], (const u16*)d_in[3], (const u16*)d_in[4],
                                                  (const u16*)d_in[5], (const u16*)d_in[6], (const u16*)d_in[7], (u16*)d_out, (char*)d_ws);
}
```

```cpp
#include <hip/hip_runtime.h>
#include <hip/hip_bf16.h>
#include <stdint.h>

typedef short bf16x8 __attribute__((ext_vector_type(8)));
typedef short s16x4 __attribute__((ext_vector_type(4)));
typedef float f32x16 __attribute__((ext_vector_type(16)));
typedef float f32x4 __attribute__((ext_vector_type(4)));
typedef unsigned u32x4 __attribute__((ext_vector_type(4)));
typedef unsigned u32x2 __attribute__((ext_vector_type(2)));
typedef __bf16 bf16x2_t __attribute__((ext_vector_type(2)));
typedef float f32x2_t __attribute__((ext_vector_type(2)));
typedef short v4i16_t __attribute__((ext_vector_type(4)));
typedef unsigned short u16;

#define LAS __attribute__((address_space(3)))
#define GAS __attribute__((address_space(1)))
#define DI __device__ __forceinline__
#define MFMA32(a, b, c) __builtin_amdgcn_mfma_f32_32x32x16_bf16((a), (b), (c), 0, 0, 0)
#define MFMA16(a, b, c) __builtin_amdgcn_mfma_f32_16x16x32_bf16((a), (b), (c), 0, 0, 0)
#define SBAR() __builtin_amdgcn_sched_barrier(0)
#define PINV(x) asm volatile("" : "+v"(x))
#define RAWBAR() asm volatile("s_barrier" ::: "memory")
#define LDSBAR() asm volatile("s_waitcnt lgkmcnt(0)\n\ts_barrier" ::: "memory")
#define DRAIN_VM() asm volatile("s_waitcnt vmcnt(0)" ::: "memory")

constexpr int VBLK = 288, SLOT = 8192 + 32 * VBLK;
constexpr unsigned WS_EPOCH = 0;
constexpr unsigned WS_TMO = 128;
constexpr unsigned FSTR = 128;
constexpr unsigned WS_FLAG_KV = 4096;
constexpr unsigned WS_FLAG_A = WS_FLAG_KV + 256 * FSTR;
constexpr unsigned WS_FLAG_B = WS_FLAG_A + 256 * FSTR;
constexpr unsigned WS_KV = WS_FLAG_B + 256 * FSTR;
constexpr unsigned WS_OA = WS_KV + 8 * SLOT;
constexpr unsigned WS_OB = WS_OA + 2097152;
constexpr unsigned WS_STAMP = WS_OB + 2097152;
constexpr unsigned WS_TOTAL = WS_STAMP + 65536 + 256 * 12 * 32;
constexpr int SC1 = 16;
#define POLLSLEEP 2
#define BPRIO 3

constexpr int LDS_BYTES = 163840;
constexpr int RB = 4 * SLOT;
constexpr int XCH = 65536;
constexpr int SYNC = RB + 81920;
constexpr int QIMG = SYNC + 1024;
enum { S_CONS = 0, S_LAND = 8, S_PRE = 16, S_BCNT = 20, S_ACNT = 24, S_PUB = 28, S_DONE1 = 32, S_AEND = 48, S_GO = 52, S_HOLD = 56, S_OBRDY = 60, S_GB2 = 64, S_XB = 68, S_VERD = 72, S_KVALL = 76 };
#define NSAMP 2
#define SHOFF 100.f
#define LMIN 9.8607613e-32f
#define BSLP 2
constexpr int QPITCH = 400;
constexpr unsigned CODE_LINES = 176;
constexpr int B1_X = 10240, B1_W = 24576, B1_ST = B1_X + B1_W;
constexpr int H_KVW = B1_ST, H_KVX = B1_ST + 16384;
static_assert(2 * B1_ST <= 81920 && H_KVX + 16384 <= 2 * B1_ST, "role B's region");
constexpr float QSCALE = 0.25f * 1.4426950408889634f;

#define STAMP(i) do { } while (0)
#define CSTAMP(i) do { } while (0)
#define STAMPDUMP() do { } while (0)

typedef __amdgpu_buffer_rsrc_t rsrc_t;

DI unsigned cvtpk(float lo, float hi) { f32x2_t v = {lo, hi}; bf16x2_t r = __builtin_convertvector(v, bf16x2_t); return __builtin_bit_cast(unsigned, r); }
DI bf16x8 as_frag(u32x4 v) { return __builtin_bit_cast(bf16x8, v); }
DI s16x4 vtr(const LAS char* p) { return __builtin_bit_cast(s16x4, __builtin_amdgcn_ds_read_tr16_b64_v4i16((LAS v4i16_t*)p)); }
DI bf16x8 trfrag(const LAS char* p0, const LAS char* p1) { s16x4 a = vtr(p0), b = vtr(p1); return __builtin_shufflevector(a, b, 0, 1, 2, 3, 4, 5, 6, 7); }
DI u32x4 ldg16(const void* p) { return *(const GAS u32x4*)p; }
DI float dot2bf(unsigned a, unsigned b, float c) { return __builtin_amdgcn_fdot2_f32_bf16(__builtin_bit_cast(bf16x2_t, a), __builtin_bit_cast(bf16x2_t, b), c, false); }
DI float swapsum(float x) { auto r = __builtin_amdgcn_permlane32_swap(__float_as_uint(x), __float_as_uint(x), false, false); return __uint_as_float(r[0]) + __uint_as_float(r[1]); }
DI float swapmax(float x) { auto r = __builtin_amdgcn_permlane32_swap(__float_as_uint(x), __float_as_uint(x), false, false); return fmaxf(__uint_as_float(r[0]), __uint_as_float(r[1])); }
DI void dma16(unsigned voff, const void* base, unsigned lds_base) {
    unsigned keep;
    asm volatile("s_mov_b32 %0, m0\n\ts_mov_b32 m0, %3\n\ts_nop 0\n\tglobal_load_lds_dwordx4 %1, %2\n\ts_mov_b32 m0, %0"
                 : "=&s"(keep) : "v"(voff), "s"(base), "s"(lds_base) : "memory");
}
DI void dma16_sc1(unsigned voff, const void* base, unsigned lds_base) {
    unsigned keep;
    asm volatile("s_mov_b32 %0, m0\n\ts_mov_b32 m0, %3\n\ts_nop 0\n\tglobal_load_lds_dwordx4 %1, %2 sc1\n\ts_mov_b32 m0, %0"
                 : "=&s"(keep) : "v"(voff), "s"(base), "s"(lds_base) : "memory");
}
DI void dma4(unsigned voff, const void* base, unsigned lds_base) {
    unsigned keep;
    asm volatile("s_mov_b32 %0, m0\n\ts_mov_b32 m0, %3\n\ts_nop 0\n\tglobal_load_lds_dwordx4 %1, %2\n\tglobal_load_lds_dwordx4 %1, %2 offset:1024\n\t"
                 "global_load_lds_dwordx4 %1, %2 offset:2048\n\tglobal_load_lds_dwordx4 %1, %2 offset:3072\n\ts_mov_b32 m0, %0"
                 : "=&s"(keep) : "v"(voff), "s"(base), "s"(lds_base) : "memory");
}
DI void dma4_sc1(unsigned voff, const void* base, unsigned lds_base) {
    unsigned keep;
    asm volatile("s_mov_b32 %0, m0\n\ts_mov_b32 m0, %3\n\ts_nop 0\n\tglobal_load_lds_dwordx4 %1, %2 sc1\n\tglobal_load_lds_dwordx4 %1, %2 offset:1024 sc1\n\t"
                 "global_load_lds_dwordx4 %1, %2 offset:2048 sc1\n\tglobal_load_lds_dwordx4 %1, %2 offset:3072 sc1\n\ts_mov_b32 m0, %0"
                 : "=&s"(keep) : "v"(voff), "s"(base), "s"(lds_base) : "memory");
}
#define PACE 1
#define PACEB() do { __builtin_amdgcn_s_sleep(PACE); SBAR(); } while (0)
DI void dmag1(unsigned v0, const void* base, unsigned lds_base) {
    asm volatile("s_mov_b32 m0, %2\n\ts_nop 0\n\tglobal_load_lds_dwordx4 %0, %1" :: "v"(v0), "s"(base), "s"(lds_base) : "memory");
}
DI void dmag2(unsigned v0, unsigned v1, const void* base, unsigned lds_base) {
    asm volatile("s_mov_b32 m0, %3\n\ts_nop 0\n\tglobal_load_lds_dwordx4 %0, %2\n\tglobal_load_lds_dwordx4 %1, %2 offset:1024" :: "v"(v0), "v"(v1), "s"(base), "s"(lds_base) : "memory");
}
DI void dmag4(unsigned v0, unsigned v1, unsigned v2, unsigned v3, const void* base, unsigned lds_base) {
    asm volatile("s_mov_b32 m0, %5\n\ts_nop 0\n\tglobal_load_lds_dwordx4 %0, %4\n\tglobal_load_lds_dwordx4 %1, %4 offset:1024\n\t"
                 "global_load_lds_dwordx4 %2, %4 offset:2048\n\tglobal_load_lds_dwordx4 %3, %4 offset:3072"
                 :: "v"(v0), "v"(v1), "v"(v2), "v"(v3), "s"(base), "s"(lds_base) : "memory");
}
DI void l2_touch(unsigned voff, const void* base, unsigned lds_corner) {
    asm volatile("s_mov_b32 m0, %2\n\ts_nop 0\n\tglobal_load_lds_dword %0, %1" :: "v"(voff), "s"(base), "s"(lds_corner) : "memory");
}
DI unsigned rfl(unsigned x) { return __builtin_amdgcn_readfirstlane(x); }
template <int OFF> DI void sig(unsigned a) { asm volatile("s_mov_b64 exec, 1\n\tds_add_u32 %0, %1 offset:%c2\n\ts_mov_b64 exec, -1" :: "v"(a), "v"(1u), "i"(OFF) : "memory"); }
template <int OFF> DI unsigned sig_rtn(unsigned a, unsigned val) {
    unsigned old;
    asm volatile("s_mov_b64 exec, 1\n\tds_add_rtn_u32 %0, %1, %2 offset:%c3\n\ts_waitcnt lgkmcnt(0)\n\ts_mov_b64 exec, -1" : "=&v"(old) : "v"(a), "v"(val), "i"(OFF) : "memory");
    return rfl(old);
}
template <int OFF> DI unsigned peek(unsigned a) { unsigned v; asm volatile("ds_read_b32 %0, %1 offset:%c2\n\ts_waitcnt lgkmcnt(0)" : "=&v"(v) : "v"(a), "i"(OFF) : "memory"); return rfl(v); }
template <int SLP, int OFF> DI void await(unsigned a, unsigned target) {
    unsigned v, sv, cnt;
    asm volatile("s_mov_b32 %2, 0x100000\n"
                 "1:\n\tds_read_b32 %0, %3 offset:%c6\n\ts_waitcnt lgkmcnt(0)\n\tv_readfirstlane_b32 %1, %0\n\ts_nop 0\n\ts_cmp_ge_u32 %1, %4\n\ts_cbranch_scc1 2f\n\t"
                 "s_sleep %5\n\ts_sub_u32 %2, %2, 1\n\ts_cbranch_scc0 1b\n"
                 "2:"
                 : "=&v"(v), "=&s"(sv), "=&s"(cnt) : "v"(a), "s"(rfl(target)), "i"(SLP), "i"(OFF) : "memory", "scc");
}
template <int OFF, int SLP = 0> DI void gbar(unsigned a, unsigned& gen, unsigned n) {
    asm volatile("s_waitcnt lgkmcnt(0)" ::: "memory");
    gen += n;
    sig<OFF>(a);
    await<SLP, OFF>(a, gen);
}

DI bool timed_out(rsrc_t rs, unsigned long long t0) {
    if (__builtin_amdgcn_s_memrealtime() - t0 > 2000000ull) {
        if ((threadIdx.x & 63) == 0) __builtin_amdgcn_raw_buffer_store_b32(1u, rs, WS_TMO, 0, SC1);
        return true;
    }
    return false;
}
DI void poll_flags(rsrc_t rs, unsigned off, bool act, unsigned tag) {
    const unsigned long long t0 = __builtin_amdgcn_s_memrealtime();
    for (;;) {
        unsigned v = tag;
        if (act) v = __builtin_amdgcn_raw_buffer_load_b32(rs, off, 0, SC1);
        if (__all(v == tag)) break;
        __builtin_amdgcn_s_sleep(POLLSLEEP);
        if (timed_out(rs, t0)) break;
    }
    asm volatile("" ::: "memory");
}
DI void poll_flags4(rsrc_t rs, unsigned off, unsigned tag) {
    const unsigned long long t0 = __builtin_amdgcn_s_memrealtime();
    for (;;) {
        const u32x4 v = __builtin_amdgcn_raw_buffer_load_b128(rs, off, 0, SC1);
        if (__all(v[0] == tag && v[1] == tag && v[2] == tag && v[3] == tag)) break;
        __builtin_amdgcn_s_sleep(POLLSLEEP);
        if (timed_out(rs, t0)) break;
    }
    asm volatile("" ::: "memory");
}

template <int NW>
DI void stage_w64(const u16* W, int col0, unsigned dst, int wave, int lane) {
    constexpr int NP = 64 / NW;
    const int rl = lane >> 3;
    const int ch0 = (lane & 7) ^ (((rl >> 1) & 1) << 2);
    const unsigned v0 = (unsigned)(((wave * NP * 8 + rl) * 512 + col0 + ch0 * 8) * 2);
#pragma unroll
    for (int i = 0; i < NP; i += 4) dmag4(v0, v0 + 7168, v0 + 14336, v0 + 21504, (const char*)W + i * 8192, rfl(dst + (wave * NP + i) * 1024));
}

DI void issue_chunk(const char* kvg, int c, unsigned dst, int i4, int lane) {
    const char* src = kvg + c * SLOT;
    dma4_sc1(lane * 16, src + i4 * 4096, rfl(dst + i4 * 4096));
    if (i4 == 0) dma16_sc1(lane * 16, src + 16384, rfl(dst + 16384));
}
DI void issue_xrow(const u16* xt, int row, unsigned dst, int lane) {
    dma16((unsigned)((lane ^ (row & 15)) << 4), (const char*)xt + row * 1024, rfl(dst + row * 1024));
}

DI float tilemax(const f32x16& s) {
    float m = fmaxf(fmaxf(s[0], s[1]), s[2]);
#pragma unroll
    for (int i = 3; i < 15; i += 2) m = fmaxf(fmaxf(m, s[i]), s[i + 1]);
    return fmaxf(m, s[15]);
}
DI void role_a(const u16* xA, const u16* wq, const u16* xB, const u16* wqkv, char* ws, rsrc_t rs, LAS char* lds, int b, int rb, int hg, int wave, unsigned& tag_out) {
    const int lane = threadIdx.x & 63;
    const int r = lane & 31, h = lane >> 5;
    const int qp = wave & 3, kh = wave >> 2;
    const unsigned lds0 = (unsigned)(uintptr_t)lds;
    const unsigned sy = lds0 + SYNC;
    const unsigned syk = sy + 4 * kh;
    const char* kvg = ws + WS_KV;
    unsigned tagv;
    STAMP(0);
    if (wave == 0 && lane < 32) *(LAS unsigned*)(lds + SYNC + lane * 4) = 0u;
    LDSBAR();
    {
        const u16* xt = xA + (size_t)rb * 64 * 512;
        const int kv0 = (hg >> 1) * 16;
#pragma unroll
        for (int i = 0; i < 6; ++i) { const int row = wave * 6 + i; issue_xrow(xt, row < kv0 ? row : row + 16, lds0 + RB, lane); }
        stage_w64<8>(wq, hg * 64, lds0, wave, lane);
        tagv = __builtin_amdgcn_raw_buffer_load_b32(rs, WS_EPOCH, 0, SC1);
        SBAR();
        DRAIN_VM();
        RAWBAR();
        STAMP(1);
        if (wave < 4) {
            const int hp = wave >> 1, ph = wave & 1;
            const int blk = (lane >> 4) & 1, q = (lane & 15) >> 2, p = lane & 3;
            const LAS char* pa = lds + (8 * h + q) * 128 + (((hp * 4 + blk * 2 + (p >> 1)) ^ (((q >> 1) & 1) << 2)) << 4) + (p & 1) * 8;
            const LAS char* xrow = lds + RB + (ph * 32 + r) * 1024;
            const LAS char* xp[8];
#pragma unroll
            for (int i = 0; i < 8; ++i) xp[i] = xrow + (((2 * i + h) ^ (r & 15)) << 4);
            f32x16 acc = {0};
            constexpr int PD = 4;
            bf16x8 xf[PD + 1], wf[PD + 1];
#pragma unroll
            for (int s = 0; s < PD; ++s) { xf[s] = *(const LAS bf16x8*)(xp[s & 7] + (s >> 3) * 256); wf[s] = trfrag(pa + s * 2048, pa + s * 2048 + 512); }
            SBAR();
#pragma unroll
            for (int s = 0; s < 32; ++s) {
                if (s + PD < 32) { xf[(s + PD) % (PD + 1)] = *(const LAS bf16x8*)(xp[(s + PD) & 7] + ((s + PD) >> 3) * 256); wf[(s + PD) % (PD + 1)] = trfrag(pa + (s + PD) * 2048, pa + (s + PD) * 2048 + 512); }
                SBAR();
                acc = MFMA32(wf[s % (PD + 1)], xf[s % (PD + 1)], acc);
                SBAR();
            }
            LAS char* dst = lds + QIMG + (hp * 128 + ph * 32 + r) * 32 + h * 16;
            *(LAS u32x4*)(dst) = (u32x4){cvtpk(acc[0] * QSCALE, acc[1] * QSCALE), cvtpk(acc[2] * QSCALE, acc[3] * QSCALE), cvtpk(acc[4] * QSCALE, acc[5] * QSCALE), cvtpk(acc[6] * QSCALE, acc[7] * QSCALE)};
            *(LAS u32x4*)(dst + 64 * 32) = (u32x4){cvtpk(acc[8] * QSCALE, acc[9] * QSCALE), cvtpk(acc[10] * QSCALE, acc[11] * QSCALE), cvtpk(acc[12] * QSCALE, acc[13] * QSCALE), cvtpk(acc[14] * QSCALE, acc[15] * QSCALE)};
        }
        STAMP(2);
        LDSBAR();
        STAMP(3);
    }
    const unsigned tag = rfl(tagv) + 1u;
    const unsigned sA = (unsigned)(2 * kh) * SLOT, sB = sA + SLOT;
    unsigned agen = 0;
    issue_chunk(kvg, 4 * kh, lds0 + sA, qp, lane);
    bf16x8 qfa = *(const LAS bf16x8*)(lds + QIMG + (qp * 64 + r) * 32 + h * 16);
    bf16x8 qfb = *(const LAS bf16x8*)(lds + QIMG + (qp * 64 + 32 + r) * 32 + h * 16);
    DRAIN_VM();
    RAWBAR();
    const int kr = h * 512 + r * 16, vr = 8192 + h * VBLK + (r < 17 ? r : 17) * 16;
    f32x16 oa, ob;
    float sha = 0.f, shb = 0.f;
    for (int attempt = 0;; ++attempt) {
        STAMP(4); CSTAMP(12);
        bf16x8 kc = *(const LAS bf16x8*)(lds + sA + kr);
        bf16x8 kn = *(const LAS bf16x8*)(lds + sA + kr + 1024);
        bf16x8 v0 = *(const LAS bf16x8*)(lds + sA + vr);
        bf16x8 v1 = *(const LAS bf16x8*)(lds + sA + vr + 2 * VBLK);
        f32x16 cia, cib, sc;
        if (attempt == 0) {
            const f32x16 z = {0};
            float ma = -INFINITY, mb = -INFINITY;
#pragma unroll
            for (int i = 0; i < NSAMP; ++i) {
                const bf16x8 k0 = *(const LAS bf16x8*)(lds + kr + i * 1024);
                ma = fmaxf(ma, tilemax(MFMA32(k0, qfa, z))); mb = fmaxf(mb, tilemax(MFMA32(k0, qfb, z)));
            }
            sha = swapmax(ma) + SHOFF;
            shb = swapmax(mb) + SHOFF;
        }
#pragma unroll
        for (int i = 0; i < 16; ++i) { cia[i] = -sha; cib[i] = -shb; }
        sc = MFMA32(kc, qfa, cia);
        oa = (f32x16){0}; ob = (f32x16){0};
        bf16x8 v1p = v1; u32x4 pp = {0u, 0u, 0u, 0u};
        unsigned s_cur = sA, s_nxt = sB;
#define EX(i) __builtin_amdgcn_exp2f(sc[i])
#define VTILE(PEND, NEXT, MINE, MID) do { \
            f32x16 sn; u32x4 p0, p1; \
            float e0 = EX(0), e1 = EX(1); PINV(e0); PINV(e1); SBAR(); \
            PEND; SBAR(); \
            float e2 = EX(2), e3 = EX(3); p0[0] = cvtpk(e0, e1); float e4 = EX(4), e5 = EX(5); p0[1] = cvtpk(e2, e3); float e6 = EX(6); \
            PINV(p0); PINV(e4); PINV(e5); PINV(e6); SBAR(); \
            sn = NEXT; PINV(sn); SBAR(); \
            float e7 = EX(7); p0[2] = cvtpk(e4, e5); float e8 = EX(8); p0[3] = cvtpk(e6, e7); PINV(p0); PINV(e8); SBAR(); \
            float e9 = EX(9), e10 = EX(10), e11 = EX(11); PINV(e9); PINV(e10); PINV(e11); SBAR(); \
            MINE; SBAR(); \
            MID; \
            float e12 = EX(12); p1[0] = cvtpk(e8, e9); float e13 = EX(13); p1[1] = cvtpk(e10, e11); float e14 = EX(14), e15 = EX(15); \
            p1[2] = cvtpk(e12, e13); p1[3] = cvtpk(e14, e15); PINV(p1); SBAR(); \
            pp = p1; sc = sn; } while (0)
        for (int j = 0; j < 4; ++j) {
            const LAS char* kcp = lds + s_cur + kr; const LAS char* vcp = lds + s_cur + vr;
            const LAS char* kxp = lds + s_nxt + kr; const LAS char* vxp = lds + s_nxt + vr;
#pragma unroll
            for (int t = 0; t < 8; ++t) {
                bf16x8 k2, nv0, nv1;
                if (t == 0) __builtin_amdgcn_s_setprio(2); else if (t == 3) __builtin_amdgcn_s_setprio(1); else if (t == 6) __builtin_amdgcn_s_setprio(0);
                VTILE({ ob = MFMA32(v1p, as_frag(pp), ob); PINV(ob); }, MFMA32(kc, qfb, cib), { oa = MFMA32(v0, as_frag(p0), oa); PINV(oa); },
                      { k2 = (t < 6) ? *(const LAS bf16x8*)(kcp + (t + 2) * 1024) : *(const LAS bf16x8*)(kxp + (t - 6) * 1024); SBAR();
                        nv1 = (t < 7) ? *(const LAS bf16x8*)(vcp + (t + 1) * 4 * VBLK + 2 * VBLK) : *(const LAS bf16x8*)(vxp + 2 * VBLK); SBAR();
                        nv0 = (t < 7) ? *(const LAS bf16x8*)(vcp + (t + 1) * 4 * VBLK) : *(const LAS bf16x8*)(vxp); SBAR(); });
                VTILE({ oa = MFMA32(v1, as_frag(pp), oa); PINV(oa); }, MFMA32(kn, qfa, cia), { ob = MFMA32(v0, as_frag(p0), ob); PINV(ob); }, { });
                v1p = v1; v1 = nv1; v0 = nv0; kc = kn; kn = k2;
                if (t == 0) {
                    if (j >= 1) sig<S_CONS>(syk);
                    else if (attempt == 0) { await<0, S_KVALL>(sy, 4); issue_chunk(kvg, 4 * kh + 1, lds0 + s_nxt, qp, lane); }
                    SBAR();
                }
                if (t == 1) {
                    if (j == 1 || j == 2) {
                        await<0, S_CONS>(syk, 12 * attempt + 4 * j);
                        issue_chunk(kvg, 4 * kh + j + 1, lds0 + s_nxt, qp, lane);
                    }
                    SBAR();
                }
                if (t == 4) { if (j < 3) { DRAIN_VM(); sig<S_LAND>(syk); } SBAR(); }
                if (t == 5) {
                    if (j < 3) await<0, S_LAND>(syk, 12 * attempt + 4 * (j + 1));
                    SBAR();
                }
            }
            const unsigned tmp = s_cur; s_cur = s_nxt; s_nxt = tmp;
        }
        ob = MFMA32(v1p, as_frag(pp), ob);
#undef EX
#undef VTILE
        __builtin_amdgcn_s_setprio(3);
        sig<S_AEND>(sy);
        STAMP(9);
        int lane2 = lane; PINV(lane2);
        const int r2 = lane2 & 31, h2 = lane2 >> 5;
        LAS char* part = lds + 2 * SLOT + qp * 4352;
        if (kh == 1) {
            await<0, S_CONS>(syk, 12 * attempt + 12);
            *(LAS f32x4*)(part + lane2 * 16) = (f32x4){oa[0], oa[1], oa[2], oa[3]};
            *(LAS f32x4*)(part + 1024 + lane2 * 16) = (f32x4){oa[4], oa[5], oa[6], oa[7]};
            *(LAS f32x4*)(part + 2176 + lane2 * 16) = (f32x4){ob[0], ob[1], ob[2], ob[3]};
            *(LAS f32x4*)(part + 2176 + 1024 + lane2 * 16) = (f32x4){ob[4], ob[5], ob[6], ob[7]};
            if (h2 == 0) { *(LAS float*)(part + 2048 + r2 * 4) = oa[8]; *(LAS float*)(part + 2176 + 2048 + r2 * 4) = ob[8]; }
            sig<S_DONE1>(sy + 4 * qp);
        } else {
            await<1, S_DONE1>(sy + 4 * qp, attempt + 1);
            const f32x4 a0 = *(const LAS f32x4*)(part + lane2 * 16), a1 = *(const LAS f32x4*)(part + 1024 + lane2 * 16);
            const f32x4 b0 = *(const LAS f32x4*)(part + 2176 + lane2 * 16), b1 = *(const LAS f32x4*)(part + 2176 + 1024 + lane2 * 16);
            const float la = oa[8] + *(const LAS float*)(part + 2048 + r2 * 4), lb = ob[8] + *(const LAS float*)(part + 2176 + 2048 + r2 * 4);
            const float sa = swapsum(h2 == 0 ? la : 0.f), sb_ = swapsum(h2 == 0 ? lb : 0.f);
            const float ra = __builtin_amdgcn_rcpf(sa), rb_ = __builtin_amdgcn_rcpf(sb_);
            float amax = 0.f;
#pragma unroll
            for (int i = 0; i < 4; ++i) {
                oa[i] += a0[i]; oa[4 + i] += a1[i]; ob[i] += b0[i]; ob[4 + i] += b1[i];
            }
            const unsigned off = WS_OA + (unsigned)((rb * 32 + hg * 4 + qp) * 64 + r2) * 32u + h2 * 16u;
            __builtin_amdgcn_raw_buffer_store_b128((u32x4){cvtpk(oa[0] * ra, oa[1] * ra), cvtpk(oa[2] * ra, oa[3] * ra), cvtpk(oa[4] * ra, oa[5] * ra), cvtpk(oa[6] * ra, oa[7] * ra)}, rs, off, 0, SC1);
            __builtin_amdgcn_raw_buffer_store_b128((u32x4){cvtpk(ob[0] * rb_, ob[1] * rb_), cvtpk(ob[2] * rb_, ob[3] * rb_), cvtpk(ob[4] * rb_, ob[5] * rb_), cvtpk(ob[6] * rb_, ob[7] * rb_)}, rs, off + 1024u, 0, SC1);
            SBAR();
            bool ok = (sa >= LMIN) && (sa <= 1.2676506e30f) && (sb_ >= LMIN) && (sb_ <= 1.2676506e30f);
#pragma unroll
            for (int i = 0; i < 8; ++i) ok = ok && (fabsf(oa[i]) <= 3.0e38f) && (fabsf(ob[i]) <= 3.0e38f);
            (void)amax;
            const bool bad = attempt == 0 && !__all(ok);
            (void)sig_rtn<S_VERD>(sy, bad ? 0x10001u : 1u);
            DRAIN_VM();
            const unsigned tot = sig_rtn<S_PUB>(sy, 1u) + 1u;
            if (tot == 4u * (attempt + 1)) {
                unsigned vd;
                for (unsigned spins = 0; ((vd = peek<S_VERD>(sy)) & 0xffffu) < 4u * (attempt + 1) && spins < (1u << 18); ++spins) { }
                if (attempt == 1 || (vd >> 16) == 0) {
                    unsigned tg = tag; PINV(tg);
                    if (lane2 == 0) __builtin_amdgcn_raw_buffer_store_b32(tg, rs, WS_FLAG_A + (rb * 8 + hg) * FSTR, 0, SC1);
                }
            }
        }
        STAMP(5); CSTAMP(13);
        unsigned pub;
        for (unsigned spins = 0; ((pub = peek<S_VERD>(sy)) & 0xffffu) < 4u * (attempt + 1) && spins < (1u << 18); ++spins) __builtin_amdgcn_s_sleep(1);
        if (attempt == 1 || (pub >> 16) == 0) break;
        float ma = -INFINITY, mb = -INFINITY;
        for (int t = 0; t < 64; ++t) {
            const u32x4 kf = __builtin_amdgcn_raw_buffer_load_b128(rs, WS_KV + (t >> 3) * SLOT + (t & 7) * 1024 + kr, 0, SC1);
            const f32x16 z = {0};
            ma = fmaxf(ma, tilemax(MFMA32(as_frag(kf), qfa, z)));
            mb = fmaxf(mb, tilemax(MFMA32(as_frag(kf), qfb, z)));
        }
        sha = swapmax(ma); shb = swapmax(mb);
        gbar<S_ACNT>(sy, agen, 8);
        issue_chunk(kvg, 4 * kh, lds0 + sA, qp, lane); issue_chunk(kvg, 4 * kh + 1, lds0 + sB, qp, lane);
        DRAIN_VM();
        gbar<S_ACNT>(sy, agen, 8);
    }
    tag_out = tag;
    if (kh == 0) STAMPDUMP();
    if (b == 0 && wave == 0) {
        const unsigned long long t0 = __builtin_amdgcn_s_memrealtime();
        for (;;) {
            unsigned v[8];
#pragma unroll
            for (int i = 0; i < 8; ++i) v[i] = __builtin_amdgcn_raw_buffer_load_b32(rs, WS_FLAG_A + (i * 64 + lane) * FSTR, 0, SC1);
            bool all = true;
#pragma unroll
            for (int i = 0; i < 8; ++i) all = all && v[i] == tag;
            if (__all(all) || timed_out(rs, t0)) break;
            __builtin_amdgcn_s_sleep(2);
        }
        if (lane == 0) __builtin_amdgcn_raw_buffer_store_b32(tag, rs, WS_EPOCH, 0, SC1);
    }
}

DI void role_b(const u16* xA, const u16* wq, const u16* xB, const u16* wqkv, const u16* wk, const u16* wv, const u16* woA, const u16* woB, u16* out,
               char* ws, rsrc_t rs, LAS char* ldsall, int b, int rb, int hg, int wave, unsigned& tag_out, unsigned& gen_out) {
    const int lane = threadIdx.x & 63;
    const int r = lane & 31, h = lane >> 5;
    const int g = lane >> 4, c16 = lane & 15;
    LAS char* lds = ldsall + RB;
    const unsigned lds0 = (unsigned)(uintptr_t)lds;
    const unsigned sy = (unsigned)(uintptr_t)ldsall + SYNC;
    unsigned tag = 0, tagv, gen = 0;
    __builtin_amdgcn_s_setprio(BPRIO);
    STAMP(0);
    const int wch = (lane & 7) ^ (((lane >> 4) & 1) << 2);
    const unsigned av0 = (unsigned)(((wave & 1) * 32 + (lane >> 3)) * 3072 + (wave >> 1) * 1024 + hg * 128 + wch * 16);
    unsigned bv[4], cv;
    {
        const int che = (lane & 7) ^ ((lane >> 4) & 7), cho = (lane & 7) ^ ((4 + (lane >> 4)) & 7);
#pragma unroll
        for (int i = 0; i < 4; ++i) {
            int gr = rb * 64 - 16 + ((wave & 1) * 4 + i) * 8 + (lane >> 3); gr = gr < 0 ? 0 : gr;
            const unsigned xo = (unsigned)(gr * 1024 + ((i & 1) ? cho : che) * 16 - i * 1024 + 4096);
            const unsigned wo = (unsigned)(((wave & 1) * 32 + i * 8 + (lane >> 3)) * 3072 + 2048 + hg * 128 + wch * 16 - i * 1024);
            bv[i] = wave < 2 ? wo : xo;
        }
        cv = wave < 2 ? (unsigned)((rb * 64 - 16 + (8 + wave) * 8 + (lane >> 3)) * 1024 + (wave ? cho : che) * 16 + 4096) : bv[0];
    }
    const char* srcA = (const char*)wqkv;
    const char* srcB = wave < 2 ? (const char*)wqkv : (const char*)xB - 4096;
    const char* srcC = (const char*)xB - 4096;
    const unsigned advB = wave < 2 ? 64 * 3072 : 128;
    const unsigned dA = B1_X + wave * 4096, dB = wave < 2 ? B1_X + 16384 + wave * 4096 : (wave - 2) * 4096, dC = wave < 2 ? (8 + wave) * 1024 : dB;
    int pfr = rb * 64 - 16 + lane; pfr = pfr < 0 ? 0 : pfr;
    const unsigned pfo = wave < 3 ? (unsigned)(lane * 3072 + wave * 1024 + hg * 128) : (unsigned)(pfr * 1024);
    const unsigned pfo2 = (unsigned)((rb * 64 + 48 + (lane & 15)) * 1024);
    auto touch_step = [&](int k) {
    };
    auto issue_step = [&](unsigned buf) {
        dmag4(av0, av0 + 23552, av0 + 47104, av0 + 70656, srcA, rfl(lds0 + buf + dA));
        dmag4(bv[0], bv[1], bv[2], bv[3], srcB, rfl(lds0 + buf + dB));
        dmag1(cv, srcC, rfl(lds0 + buf + dC));
        srcA += 64 * 3072; srcB += advB; srcC += 128;
    };
    {
        const int which = hg & 1, kv0 = (hg >> 1) * 16, t16 = rb * 4 + (hg >> 1);
        const u16* xt = xA + (size_t)rb * 64 * 512;
        {
            const u16* W = which ? wv : wk;
            dma4(lane * 16, (const char*)W + wave * 4096, rfl(lds0 + XCH + wave * 4096));
#pragma unroll
            for (int i = 0; i < 4; ++i) issue_xrow(xt, kv0 + wave * 4 + i, lds0, lane);
            tagv = __builtin_amdgcn_raw_buffer_load_b32(rs, WS_EPOCH, 0, SC1);
            SBAR();
        }
        RAWBAR();
        DRAIN_VM();
        gbar<S_BCNT>(sy, gen, 4);
        tag = rfl(tagv) + 1u;
        f32x4 acc;
        {
            STAMP(7);
            const LAS char* base = lds + XCH + (8 * g + (c16 >> 2)) * 32 + (lane & 3) * 8 + wave * 4096;
            const LAS char* xrow = lds + (kv0 + c16) * 1024 + wave * 256;
            bf16x8 xf[4], wf[4];
#pragma unroll
            for (int s = 0; s < 4; ++s) { xf[s] = *(const LAS bf16x8*)(xrow + (((4 * s + g) ^ c16) << 4)); wf[s] = trfrag(base + s * 1024, base + s * 1024 + 128); }
            f32x4 a1 = {0, 0, 0, 0}, a2 = {0, 0, 0, 0};
            if (which == 0) { a1 = MFMA16(wf[0], xf[0], a1); a2 = MFMA16(wf[1], xf[1], a2); a1 = MFMA16(wf[2], xf[2], a1); a2 = MFMA16(wf[3], xf[3], a2); }
            else { a1 = MFMA16(xf[0], wf[0], a1); a2 = MFMA16(xf[1], wf[1], a2); a1 = MFMA16(xf[2], wf[2], a1); a2 = MFMA16(xf[3], wf[3], a2); }
            acc = a1 + a2;
            if (wave != 0) *(LAS f32x4*)(ldsall + QIMG + wave * 1024 + lane * 16) = acc;
            gbar<S_BCNT>(sy, gen, 4);
        }
        if (wave == 0) {
#pragma unroll
            for (int i = 1; i < 4; ++i) acc += *(const LAS f32x4*)(ldsall + QIMG + i * 1024 + lane * 16);
            const u32x2 w = {cvtpk(acc[0], acc[1]), cvtpk(acc[2], acc[3])};
            const int t32 = t16 >> 1, s16 = t16 & 1, t = t32 & 7;
            const unsigned cbase = WS_KV + (t32 >> 3) * SLOT;
            const unsigned vb = cbase + 8192 + (t * 2 + s16) * 2 * VBLK;
            const unsigned off = which ? vb + (g & 1) * VBLK + c16 * 16 + (g >> 1) * 8 : cbase + t * 1024 + (g & 1) * 512 + (s16 * 16 + c16) * 16 + (g >> 1) * 8;
            __builtin_amdgcn_raw_buffer_store_b64(w, rs, off, 0, SC1);
            if (which && lane < 8) {
                const unsigned val = (lane & 2) ? 0u : 0x3f803f80u;
                __builtin_amdgcn_raw_buffer_store_b64((u32x2){val, val}, rs, vb + (lane >> 2) * VBLK + 256 + (lane & 3) * 8, 0, SC1);
            }
            DRAIN_VM();
            if (lane == 0) __builtin_amdgcn_raw_buffer_store_b32(tag, rs, WS_FLAG_KV + (rb * 8 + hg) * FSTR, 0, SC1);
            STAMP(8);
        }
        RAWBAR();
        if (wave == 0) poll_flags(rs, WS_FLAG_KV + ((lane & 31) + (lane >> 5) * 128) * FSTR, true, tag);
        STAMP(9);
        RAWBAR();
        RAWBAR();
        poll_flags(rs, WS_FLAG_KV + (wave * 64 + lane) * FSTR, true, tag);
        sig<S_KVALL>(sy);
        issue_step(0);
    }
    {
        const int rtA = (wave == 0) ? 0 : (wave == 3 ? 2 : 1), rtB = (wave == 0) ? 0 : (wave == 1 ? 1 : 2);
        const int sgA = (wave == 0) ? 1 : (wave == 2 ? 2 : 0), sgB = (wave == 0) ? 2 : (wave == 2 ? 2 : 1);
        int irA = rtA * 32 - 16 + r; irA = irA < 0 ? 0 : irA;
        int irB = rtB * 32 - 16 + r; irB = irB < 0 ? 0 : irB;
        int xa[4], xb_[4];
#pragma unroll
        for (int i = 0; i < 4; ++i) { xa[i] = irA * 128 + (((2 * i + h) ^ ((irA >> 1) & 7)) << 4); xb_[i] = irB * 128 + (((2 * i + h) ^ ((irB >> 1) & 7)) << 4); }
        const int blk = (lane >> 4) & 1, q = (lane & 15) >> 2, p = lane & 3;
        const int ch0 = (blk * 2 + (p >> 1)) ^ (((q >> 1) & 1) << 2);
        const int rdl = B1_X + (8 * h + q) * 128 + (p & 1) * 8;
        const int a0 = rdl + sgA * 8192 + ch0 * 16, a1 = rdl + sgA * 8192 + (ch0 ^ 4) * 16;
        const int b0 = rdl + sgB * 8192 + ch0 * 16, b1 = rdl + sgB * 8192 + (ch0 ^ 4) * 16;
        f32x16 acc0 = {0}, acc1 = {0}, acc2 = {0}, acc3 = {0};
        unsigned buf = 0;
#pragma unroll 1
        for (int k = 0; k < 8; ++k) {
            DRAIN_VM();
            gbar<S_BCNT, BSLP>(sy, gen, 4);
            const bool paced = true;
            if (k < 7) issue_step(B1_ST - buf);
            if (k < 6) touch_step(k + 2);
            if (k == 5) {
                l2_touch((unsigned)((wave * 128 + lane) * 1024 + hg * 128), woB, sy + 640); l2_touch((unsigned)((wave * 128 + 64 + lane) * 1024 + hg * 128), woB, sy + 640);
            }
            const LAS char* sb = lds + buf;
#pragma unroll
            for (int hs = 0; hs < 2; ++hs) {
                bf16x8 fx[2][2], fw[2][4];
#pragma unroll
                for (int s = 0; s < 2; ++s) {
                    const int ks = 2 * hs + s;
                    fx[s][0] = *(const LAS bf16x8*)(sb + xa[ks]);
                    fw[s][0] = trfrag(sb + a0 + ks * 2048, sb + a0 + ks * 2048 + 512); fw[s][1] = trfrag(sb + a1 + ks * 2048, sb + a1 + ks * 2048 + 512);
                    fx[s][1] = *(const LAS bf16x8*)(sb + xb_[ks]);
                    fw[s][2] = trfrag(sb + b0 + ks * 2048, sb + b0 + ks * 2048 + 512); fw[s][3] = trfrag(sb + b1 + ks * 2048, sb + b1 + ks * 2048 + 512);
                }
                SBAR();
#define BSTEP(ACC, FW, FX) do { ACC = MFMA32(FW, FX, ACC); PINV(ACC); SBAR(); if (paced) __builtin_amdgcn_s_sleep(PACE); SBAR(); } while (0)
                BSTEP(acc0, fw[0][0], fx[0][0]); BSTEP(acc1, fw[0][1], fx[0][0]); BSTEP(acc2, fw[0][2], fx[0][1]); BSTEP(acc3, fw[0][3], fx[0][1]);
                BSTEP(acc0, fw[1][0], fx[1][0]); BSTEP(acc1, fw[1][1], fx[1][0]); BSTEP(acc2, fw[1][2], fx[1][1]); BSTEP(acc3, fw[1][3], fx[1][1]);
#undef BSTEP
            }
            buf = B1_ST - buf;
        }
        gbar<S_BCNT>(sy, gen, 4);
        STAMP(2);
#define PK8(A, o) (u32x4){cvtpk(A[o], A[o + 1]), cvtpk(A[o + 2], A[o + 3]), cvtpk(A[o + 4], A[o + 5]), cvtpk(A[o + 6], A[o + 7])}
        if (rtA != 0 || r >= 16) {
            LAS char* qa = lds + (rtA * 32 - 16 + r) * QPITCH + sgA * 128 + h * 16;
            *(LAS u32x4*)(qa + 0) = PK8(acc0, 0); *(LAS u32x4*)(qa + 32) = PK8(acc0, 8); *(LAS u32x4*)(qa + 64) = PK8(acc1, 0); *(LAS u32x4*)(qa + 96) = PK8(acc1, 8);
            LAS char* qb = lds + (rtB * 32 - 16 + r) * QPITCH + sgB * 128 + h * 16;
            *(LAS u32x4*)(qb + 0) = PK8(acc2, 0); *(LAS u32x4*)(qb + 32) = PK8(acc2, 8); *(LAS u32x4*)(qb + 64) = PK8(acc3, 0); *(LAS u32x4*)(qb + 96) = PK8(acc3, 8);
        }
#undef PK8
        gbar<S_BCNT>(sy, gen, 4);
    }
#pragma unroll 1
    for (int ph = 0; ph < 2; ++ph) {
        const int pos = ph * 32 + r;
        const LAS char* row = lds + (16 + pos) * QPITCH + wave * 32;
        const u32x4 q0 = *(const LAS u32x4*)(row), q1 = *(const LAS u32x4*)(row + 16);
        const LAS char* krow = row + 128 - 8 * h * QPITCH;
        float sc[4];
#pragma unroll
        for (int i = 0; i < 4; ++i) {
            const u32x4 k0 = *(const LAS u32x4*)(krow - 2 * i * QPITCH), k1 = *(const LAS u32x4*)(krow - 2 * i * QPITCH + 16);
            float s = 0.f, s2 = 0.f;
#pragma unroll
            for (int j = 0; j < 4; ++j) { s = dot2bf(q0[j], k0[j], s); s2 = dot2bf(q1[j], k1[j], s2); }
            sc[i] = (rb * 64 + pos - 2 * (4 * h + i) >= 0) ? (s + s2) * QSCALE : -INFINITY;
        }
        const float m = swapmax(fmaxf(fmaxf(sc[0], sc[1]), fmaxf(sc[2], sc[3])));
        float pw[4];
#pragma unroll
        for (int i = 0; i < 4; ++i) pw[i] = __builtin_amdgcn_exp2f(sc[i] - m);
        const float l = swapsum((pw[0] + pw[1]) + (pw[2] + pw[3]));
        const unsigned pa = cvtpk(pw[0], pw[1]), pb = cvtpk(pw[2], pw[3]);
        const auto ra = __builtin_amdgcn_permlane32_swap(pa, pa, false, false);
        const auto rb2 = __builtin_amdgcn_permlane32_swap(pb, pb, false, false);
        const unsigned pp[4] = {ra[0], rb2[0], ra[1], rb2[1]};
        float o[8];
#pragma unroll
        for (int j = 0; j < 8; ++j) o[j] = 0.f;
        const LAS char* vrow = row + 256 + h * 16;
#pragma unroll
        for (int w2 = 0; w2 < 4; ++w2) {
            const u32x4 va = *(const LAS u32x4*)(vrow - 4 * w2 * QPITCH), vb = *(const LAS u32x4*)(vrow - (4 * w2 + 2) * QPITCH);
#pragma unroll
            for (int j = 0; j < 4; ++j) {
                o[2 * j] = dot2bf(__builtin_amdgcn_perm(vb[j], va[j], 0x05040100u), pp[w2], o[2 * j]);
                o[2 * j + 1] = dot2bf(__builtin_amdgcn_perm(vb[j], va[j], 0x07060302u), pp[w2], o[2 * j + 1]);
            }
        }
        const float rl = __builtin_amdgcn_rcpf(l);
        u32x4 ow = {cvtpk(o[0] * rl, o[1] * rl), cvtpk(o[2] * rl, o[3] * rl), cvtpk(o[4] * rl, o[5] * rl), cvtpk(o[6] * rl, o[7] * rl)};
        __builtin_amdgcn_raw_buffer_store_b128(ow, rs, WS_OB + (unsigned)((rb * 32 + hg * 4 + wave) * 64 + pos) * 32u + h * 16u, 0, SC1);
    }
    DRAIN_VM();
    gbar<S_BCNT>(sy, gen, 4);
    if (wave == 0 && lane == 0) __builtin_amdgcn_raw_buffer_store_b32(tag, rs, WS_FLAG_B + (rb * 8 + hg) * FSTR, 0, SC1);
    STAMP(3);
    stage_w64<4>(woB, hg * 64, lds0, wave, lane);
#pragma unroll
    for (int i = 0; i < 2; ++i)
        asm volatile("s_mov_b32 m0, %2\n\ts_nop 0\n\tglobal_load_lds_dword %0, %1" :: "v"((unsigned)(((wave * 2 + i) * 64 + lane) * 1024 + hg * 128)), "s"(woA), "s"(sy + 640) : "memory");
    DRAIN_VM();
    gbar<S_BCNT, 1>(sy, gen, 4);
    if (wave == 0) { poll_flags(rs, WS_FLAG_B + (rb * 8 + (lane & 7)) * FSTR, lane < 8, tag); sig<S_OBRDY>(sy); }
    STAMP(4);
    {
        unsigned pub;
        for (unsigned spins = 0; spins < (1u << 18); ++spins) {
            pub = peek<S_VERD>(sy);
            if ((pub & 0xffffu) >= 8u || ((pub & 0xffffu) == 4u && (pub >> 16) == 0)) break;
            __builtin_amdgcn_s_sleep(2);
        }
    }
    stage_w64<4>(woA, hg * 64, (unsigned)(uintptr_t)ldsall, wave, lane);
    DRAIN_VM();
    gbar<S_BCNT>(sy, gen, 4);
    STAMP(5);
    tag_out = tag; gen_out = gen;
}

constexpr int OPD = 6;
DI void out_tile(int br, int wave, unsigned tag, unsigned gen, u16* out, char* ws, rsrc_t rs, LAS char* ldsall, int rb, int hg) {
    const int lane = threadIdx.x & 63;
    const int r = lane & 31, h = lane >> 5;
    const unsigned sy = (unsigned)(uintptr_t)ldsall + SYNC;
    const int kh = wave & 1, ph = wave >> 1;
    const int blk = (lane >> 4) & 1, q = (lane & 15) >> 2, p = lane & 3;
    const int ch0 = (blk * 2 + (p >> 1)) ^ (((q >> 1) & 1) << 2);
    const LAS char* rowp = ldsall + (br ? RB : 0) + (kh * 256 + 4 * h + q) * 128 + (p & 1) * 8;
    const LAS char* b0 = rowp + ch0 * 16; const LAS char* b1 = rowp + (ch0 ^ 4) * 16;
    if (br) await<1, S_OBRDY>(sy, 1);
    bf16x8 w0[16], w1[16];
#pragma unroll
    for (int s = 0; s < OPD; ++s) { w0[s] = trfrag(b0 + s * 2048, b0 + s * 2048 + 1024); w1[s] = trfrag(b1 + s * 2048, b1 + s * 2048 + 1024); }
#pragma unroll
    for (int s = 0; s < OPD; ++s) { PINV(w0[s]); PINV(w1[s]); }
    if (!br) poll_flags(rs, WS_FLAG_A + (rb * 8 + (lane & 7)) * FSTR, lane < 8, tag);
    STAMP(6);
    u32x4 bfr[16];
    const unsigned obase = (br ? WS_OB : WS_OA) + (unsigned)((rb * 32 + kh * 16) * 64 + ph * 32 + r) * 32u + h * 16u;
#pragma unroll
    for (int s = 0; s < 16; ++s) bfr[s] = __builtin_amdgcn_raw_buffer_load_b128(rs, obase + s * 2048u, 0, SC1);
    SBAR();
    f32x16 acc0 = {0}, acc1 = {0};
#pragma unroll
    for (int s = 0; s < 16; ++s) {
        acc0 = MFMA32(w0[s], as_frag(bfr[s]), acc0);
        acc1 = MFMA32(w1[s], as_frag(bfr[s]), acc1);
        if (s + OPD < 16) { w0[s + OPD] = trfrag(b0 + (s + OPD) * 2048, b0 + (s + OPD) * 2048 + 1024); w1[s + OPD] = trfrag(b1 + (s + OPD) * 2048, b1 + (s + OPD) * 2048 + 1024); }
        SBAR();
    }
    LAS f32x4* mine = (LAS f32x4*)(ldsall + RB + XCH + wave * 4096);
    const LAS f32x4* theirs = (const LAS f32x4*)(ldsall + RB + XCH + (wave ^ 1) * 4096);
    const f32x16 give = kh ? acc0 : acc1, keep = kh ? acc1 : acc0;
    if (!br) await<0, S_XB>(sy, 4);
#pragma unroll
    for (int i = 0; i < 4; ++i) mine[i * 64 + lane] = (f32x4){give[4 * i], give[4 * i + 1], give[4 * i + 2], give[4 * i + 3]};
    gbar<0>(sy + (br ? S_GB2 : S_BCNT), gen, 4);
    f32x4 t[4];
#pragma unroll
    for (int i = 0; i < 4; ++i) t[i] = theirs[i * 64 + lane];
    if (br) { asm volatile("s_waitcnt lgkmcnt(0)" ::: "memory"); sig<S_XB>(sy); }
    unsigned w[8];
#pragma unroll
    for (int i = 0; i < 4; ++i) { w[2 * i] = cvtpk(keep[4 * i] + t[i][0], keep[4 * i + 1] + t[i][1]); w[2 * i + 1] = cvtpk(keep[4 * i + 2] + t[i][2], keep[4 * i + 3] + t[i][3]); }
    u16* dst = out + (size_t)(rb * 64 + ph * 32 + r) * 1024 + br * 512 + hg * 64 + kh * 32 + h * 8;
#pragma unroll
    for (int i = 0; i < 2; ++i) {
        auto r0 = __builtin_amdgcn_permlane32_swap(w[4 * i], w[4 * i + 2], false, false);
        auto r1 = __builtin_amdgcn_permlane32_swap(w[4 * i + 1], w[4 * i + 3], false, false);
        *(GAS u32x4*)(dst + i * 16) = (u32x4){r0[0], r1[0], r0[1], r1[1]};
    }
    STAMP(10);
    STAMPDUMP();
}

__global__ void __launch_bounds__(768, 3) fused_kernel(const u16* xA, const u16* wq, const u16* wk, const u16* wv, const u16* woA,
                                                       const u16* xB, const u16* wqkv, const u16* woB, u16* out, char* ws) {
    extern __shared__ __attribute__((aligned(16))) char smem[];
    LAS char* lds = (LAS char*)smem;
    const rsrc_t rs = __builtin_amdgcn_make_buffer_rsrc(ws, 0, WS_TOTAL, 0x00020000);
    const int b = blockIdx.x;
    const int rb = ((b & 7) >> 1) * 8 + ((b >> 3) & 7), hg = (b & 1) * 4 + (b >> 6);
    const int wave = __builtin_amdgcn_readfirstlane(threadIdx.x >> 6);
    unsigned tag = 0, gen = 0;
    if (wave < 8) role_a(xA, wq, xB, wqkv, ws, rs, lds, b, rb, hg, wave, tag);
    else role_b(xA, wq, xB, wqkv, wk, wv, woA, woB, out, ws, rs, lds, b, rb, hg, wave - 8, tag, gen);
    if (wave >= 4) out_tile(wave < 8 ? 1 : 0, wave & 3, tag, gen, out, ws, rs, lds, rb, hg);
}

extern "C" void kernel_launch(void* const* d_in, const int* in_sizes, int n_in, void* d_out, int out_size, void* d_ws, size_t ws_size,
                              hipStream_t stream) {
    (void)in_sizes; (void)n_in; (void)out_size; (void)ws_size;
    (void)hipFuncSetAttribute((const void*)fused_kernel, hipFuncAttributeMaxDynamicSharedMemorySize, LDS_BYTES);
    fused_kernel<<<256, 768, LDS_BYTES, stream>>>((const u16*)d_in[0], (const u16*)d_in[1], (const u16*)d_in[2], (const u16*)d_in[3], (const u16*)d_in[4],
                                                  (const u16*)d_in[5], (const u16*)d_in[6], (const u16*)d_in[7], (u16*)d_out, (char*)d_ws);
}
```

```cpp
#include <hip/hip_runtime.h>
#include <hip/hip_bf16.h>
#include <stdint.h>

typedef short bf16x8 __attribute__((ext_vector_type(8)));
typedef short s16x4 __attribute__((ext_vector_type(4)));
typedef float f32x16 __attribute__((ext_vector_type(16)));
typedef float f32x4 __attribute__((ext_vector_type(4)));
typedef unsigned u32x4 __attribute__((ext_vector_type(4)));
typedef unsigned u32x2 __attribute__((ext_vector_type(2)));
typedef __bf16 bf16x2_t __attribute__((ext_vector_type(2)));
typedef float f32x2_t __attribute__((ext_vector_type(2)));
typedef short v4i16_t __attribute__((ext_vector_type(4)));
typedef unsigned short u16;

#define LAS __attribute__((address_space(3)))
#define GAS __attribute__((address_space(1)))
#define DI __device__ __forceinline__
#define MFMA32(a, b, c) __builtin_amdgcn_mfma_f32_32x32x16_bf16((a), (b), (c), 0, 0, 0)
#define MFMA16(a, b, c) __builtin_amdgcn_mfma_f32_16x16x32_bf16((a), (b), (c), 0, 0, 0)
#define SBAR() __builtin_amdgcn_sched_barrier(0)
#define PINV(x) asm volatile("" : "+v"(x))
#define RAWBAR() asm volatile("s_barrier" ::: "memory")
#define LDSBAR() asm volatile("s_waitcnt lgkmcnt(0)\n\ts_barrier" ::: "memory")
#define DRAIN_VM() asm volatile("s_waitcnt vmcnt(0)" ::: "memory")

constexpr int VBLK = 288, SLOT = 8192 + 32 * VBLK;
constexpr unsigned WS_EPOCH = 0;
constexpr unsigned WS_TMO = 128;
constexpr unsigned FSTR = 128;
constexpr unsigned WS_FLAG_KV = 4096;
constexpr unsigned WS_FLAG_A = WS_FLAG_KV + 256 * FSTR;
constexpr unsigned WS_FLAG_B = WS_FLAG_A + 256 * FSTR;
constexpr unsigned WS_KV = WS_FLAG_B + 256 * FSTR;
constexpr unsigned WS_OA = WS_KV + 8 * SLOT;
constexpr unsigned WS_OB = WS_OA + 2097152;
constexpr unsigned WS_STAMP = WS_OB + 2097152;
constexpr unsigned WS_TOTAL = WS_STAMP + 65536 + 256 * 12 * 32;
constexpr int SC1 = 16;
#define POLLSLEEP 2
#define BPRIO 3

constexpr int LDS_BYTES = 163840;
constexpr int RB = 4 * SLOT;
constexpr int XCH = 65536;
constexpr int SYNC = RB + 81920;
constexpr int QIMG = SYNC + 1024;
enum { S_CONS = 0, S_LAND = 8, S_PRE = 16, S_BCNT = 20, S_ACNT = 24, S_PUB = 28, S_DONE1 = 32, S_AEND = 48, S_GO = 52, S_HOLD = 56, S_OBRDY = 60, S_GB2 = 64, S_XB = 68, S_VERD = 72, S_KVALL = 76 };
#define LADDER 0
#define ZROW 80
#define NSAMP 2
#define SHOFF 100.f
#define LMIN 9.8607613e-32f
#define BSLP 2
constexpr int QPITCH = 400;
constexpr unsigned CODE_LINES = 176;
constexpr int B1_X = 10496, B1_W = 24576, B1_ST = B1_X + B1_W;
constexpr int H_KVW = B1_ST, H_KVX = B1_ST + 16384;
static_assert(2 * B1_ST <= 81920 && H_KVX + 16384 <= 2 * B1_ST, "role B's region");
constexpr float QSCALE = 0.25f * 1.4426950408889634f;

#define STAMP(i) do { } while (0)
#define CSTAMP(i) do { } while (0)
#define STAMPDUMP() do { } while (0)

typedef __amdgpu_buffer_rsrc_t rsrc_t;

DI unsigned cvtpk(float lo, float hi) { f32x2_t v = {lo, hi}; bf16x2_t r = __builtin_convertvector(v, bf16x2_t); return __builtin_bit_cast(unsigned, r); }
DI bf16x8 as_frag(u32x4 v) { return __builtin_bit_cast(bf16x8, v); }
DI s16x4 vtr(const LAS char* p) { return __builtin_bit_cast(s16x4, __builtin_amdgcn_ds_read_tr16_b64_v4i16((LAS v4i16_t*)p)); }
DI bf16x8 trfrag(const LAS char* p0, const LAS char* p1) { s16x4 a = vtr(p0), b = vtr(p1); return __builtin_shufflevector(a, b, 0, 1, 2, 3, 4, 5, 6, 7); }
DI u32x4 ldg16(const void* p) { return *(const GAS u32x4*)p; }
DI float dot2bf(unsigned a, unsigned b, float c) { return __builtin_amdgcn_fdot2_f32_bf16(__builtin_bit_cast(bf16x2_t, a), __builtin_bit_cast(bf16x2_t, b), c, false); }
DI float swapsum(float x) { auto r = __builtin_amdgcn_permlane32_swap(__float_as_uint(x), __float_as_uint(x), false, false); return __uint_as_float(r[0]) + __uint_as_float(r[1]); }
DI float swapmax(float x) { auto r = __builtin_amdgcn_permlane32_swap(__float_as_uint(x), __float_as_uint(x), false, false); return fmaxf(__uint_as_float(r[0]), __uint_as_float(r[1])); }
DI void dma16(unsigned voff, const void* base, unsigned lds_base) {
    unsigned keep;
    asm volatile("s_mov_b32 %0, m0\n\ts_mov_b32 m0, %3\n\ts_nop 0\n\tglobal_load_lds_dwordx4 %1, %2\n\ts_mov_b32 m0, %0"
                 : "=&s"(keep) : "v"(voff), "s"(base), "s"(lds_base) : "memory");
}
DI void dma16_sc1(unsigned voff, const void* base, unsigned lds_base) {
    unsigned keep;
    asm volatile("s_mov_b32 %0, m0\n\ts_mov_b32 m0, %3\n\ts_nop 0\n\tglobal_load_lds_dwordx4 %1, %2 sc1\n\ts_mov_b32 m0, %0"
                 : "=&s"(keep) : "v"(voff), "s"(base), "s"(lds_base) : "memory");
}
DI void dma4(unsigned voff, const void* base, unsigned lds_base) {
    unsigned keep;
    asm volatile("s_mov_b32 %0, m0\n\ts_mov_b32 m0, %3\n\ts_nop 0\n\tglobal_load_lds_dwordx4 %1, %2\n\tglobal_load_lds_dwordx4 %1, %2 offset:1024\n\t"
                 "global_load_lds_dwordx4 %1, %2 offset:2048\n\tglobal_load_lds_dwordx4 %1, %2 offset:3072\n\ts_mov_b32 m0, %0"
                 : "=&s"(keep) : "v"(voff), "s"(base), "s"(lds_base) : "memory");
}
DI void dma4_sc1(unsigned voff, const void* base, unsigned lds_base) {
    unsigned keep;
    asm volatile("s_mov_b32 %0, m0\n\ts_mov_b32 m0, %3\n\ts_nop 0\n\tglobal_load_lds_dwordx4 %1, %2 sc1\n\tglobal_load_lds_dwordx4 %1, %2 offset:1024 sc1\n\t"
                 "global_load_lds_dwordx4 %1, %2 offset:2048 sc1\n\tglobal_load_lds_dwordx4 %1, %2 offset:3072 sc1\n\ts_mov_b32 m0, %0"
                 : "=&s"(keep) : "v"(voff), "s"(base), "s"(lds_base) : "memory");
}
#define PACE 1
#define PACEB() do { __builtin_amdgcn_s_sleep(PACE); SBAR(); } while (0)
DI void dmag1(unsigned v0, const void* base, unsigned lds_base) {
    asm volatile("s_mov_b32 m0, %2\n\ts_nop 0\n\tglobal_load_lds_dwordx4 %0, %1" :: "v"(v0), "s"(base), "s"(lds_base) : "memory");
}
DI void dmag2(unsigned v0, unsigned v1, const void* base, unsigned lds_base) {
    asm volatile("s_mov_b32 m0, %3\n\ts_nop 0\n\tglobal_load_lds_dwordx4 %0, %2\n\tglobal_load_lds_dwordx4 %1, %2 offset:1024" :: "v"(v0), "v"(v1), "s"(base), "s"(lds_base) : "memory");
}
DI void dmag4(unsigned v0, unsigned v1, unsigned v2, unsigned v3, const void* base, unsigned lds_base) {
    asm volatile("s_mov_b32 m0, %5\n\ts_nop 0\n\tglobal_load_lds_dwordx4 %0, %4\n\tglobal_load_lds_dwordx4 %1, %4 offset:1024\n\t"
                 "global_load_lds_dwordx4 %2, %4 offset:2048\n\tglobal_load_lds_dwordx4 %3, %4 offset:3072"
                 :: "v"(v0), "v"(v1), "v"(v2), "v"(v3), "s"(base), "s"(lds_base) : "memory");
}
DI void l2_touch(unsigned voff, const void* base, unsigned lds_corner) {
    asm volatile("s_mov_b32 m0, %2\n\ts_nop 0\n\tglobal_load_lds_dword %0, %1" :: "v"(voff), "s"(base), "s"(lds_corner) : "memory");
}
DI unsigned rfl(unsigned x) { return __builtin_amdgcn_readfirstlane(x); }
template <int OFF> DI void sig(unsigned a) { asm volatile("s_mov_b64 exec, 1\n\tds_add_u32 %0, %1 offset:%c2\n\ts_mov_b64 exec, -1" :: "v"(a), "v"(1u), "i"(OFF) : "memory"); }
template <int OFF> DI unsigned sig_rtn(unsigned a, unsigned val) {
    unsigned old;
    asm volatile("s_mov_b64 exec, 1\n\tds_add_rtn_u32 %0, %1, %2 offset:%c3\n\ts_waitcnt lgkmcnt(0)\n\ts_mov_b64 exec, -1" : "=&v"(old) : "v"(a), "v"(val), "i"(OFF) : "memory");
    return rfl(old);
}
template <int OFF> DI unsigned peek(unsigned a) { unsigned v; asm volatile("ds_read_b32 %0, %1 offset:%c2\n\ts_waitcnt lgkmcnt(0)" : "=&v"(v) : "v"(a), "i"(OFF) : "memory"); return rfl(v); }
template <int SLP, int OFF> DI void await(unsigned a, unsigned target) {
    unsigned v, sv, cnt;
    asm volatile("s_mov_b32 %2, 0x100000\n"
                 "1:\n\tds_read_b32 %0, %3 offset:%c6\n\ts_waitcnt lgkmcnt(0)\n\tv_readfirstlane_b32 %1, %0\n\ts_nop 0\n\ts_cmp_ge_u32 %1, %4\n\ts_cbranch_scc1 2f\n\t"
                 "s_sleep %5\n\ts_sub_u32 %2, %2, 1\n\ts_cbranch_scc0 1b\n"
                 "2:"
                 : "=&v"(v), "=&s"(sv), "=&s"(cnt) : "v"(a), "s"(rfl(target)), "i"(SLP), "i"(OFF) : "memory", "scc");
}
template <int OFF, int SLP = 0> DI void gbar(unsigned a, unsigned& gen, unsigned n) {
    asm volatile("s_waitcnt lgkmcnt(0)" ::: "memory");
    gen += n;
    sig<OFF>(a);
    await<SLP, OFF>(a, gen);
}

DI bool timed_out(rsrc_t rs, unsigned long long t0) {
    if (__builtin_amdgcn_s_memrealtime() - t0 > 2000000ull) {
        if ((threadIdx.x & 63) == 0) __builtin_amdgcn_raw_buffer_store_b32(1u, rs, WS_TMO, 0, SC1);
        return true;
    }
    return false;
}
DI void poll_flags(rsrc_t rs, unsigned off, bool act, unsigned tag) {
    const unsigned long long t0 = __builtin_amdgcn_s_memrealtime();
    for (;;) {
        unsigned v = tag;
        if (act) v = __builtin_amdgcn_raw_buffer_load_b32(rs, off, 0, SC1);
        if (__all(v == tag)) break;
        __builtin_amdgcn_s_sleep(POLLSLEEP);
        if (timed_out(rs, t0)) break;
    }
    asm volatile("" ::: "memory");
}
DI void poll_flags4(rsrc_t rs, unsigned off, unsigned tag) {
    const unsigned long long t0 = __builtin_amdgcn_s_memrealtime();
    for (;;) {
        const u32x4 v = __builtin_amdgcn_raw_buffer_load_b128(rs, off, 0, SC1);
        if (__all(v[0] == tag && v[1] == tag && v[2] == tag && v[3] == tag)) break;
        __builtin_amdgcn_s_sleep(POLLSLEEP);
        if (timed_out(rs, t0)) break;
    }
    asm volatile("" ::: "memory");
}

template <int NW>
DI void stage_w64(const u16* W, int col0, unsigned dst, int wave, int lane) {
    constexpr int NP = 64 / NW;
    const int rl = lane >> 3;
    const int ch0 = (lane & 7) ^ (((rl >> 1) & 1) << 2);
    const unsigned v0 = (unsigned)(((wave * NP * 8 + rl) * 512 + col0 + ch0 * 8) * 2);
#pragma unroll
    for (int i = 0; i < NP; i += 4) dmag4(v0, v0 + 7168, v0 + 14336, v0 + 21504, (const char*)W + i * 8192, rfl(dst + (wave * NP + i) * 1024));
}

DI void issue_chunk(const char* kvg, int c, unsigned dst, int i4, int lane) {
    const char* src = kvg + c * SLOT;
    dma4_sc1(lane * 16, src + i4 * 4096, rfl(dst + i4 * 4096));
    if (i4 == 0) dma16_sc1(lane * 16, src + 16384, rfl(dst + 16384));
}
DI void issue_xrow(const u16* xt, int row, unsigned dst, int lane) {
    dma16((unsigned)((lane ^ (row & 15)) << 4), (const char*)xt + row * 1024, rfl(dst + row * 1024));
}

DI float tilemax(const f32x16& s) {
    float m = fmaxf(fmaxf(s[0], s[1]), s[2]);
#pragma unroll
    for (int i = 3; i < 15; i += 2) m = fmaxf(fmaxf(m, s[i]), s[i + 1]);
    return fmaxf(m, s[15]);
}
DI void role_a(const u16* xA, const u16* wq, const u16* xB, const u16* wqkv, char* ws, rsrc_t rs, LAS char* lds, int b, int rb, int hg, int wave, unsigned& tag_out) {
    const int lane = threadIdx.x & 63;
    const int r = lane & 31, h = lane >> 5;
    const int qp = wave & 3, kh = wave >> 2;
    const unsigned lds0 = (unsigned)(uintptr_t)lds;
    const unsigned sy = lds0 + SYNC;
    const unsigned syk = sy + 4 * kh;
    const char* kvg = ws + WS_KV;
    unsigned tagv;
    STAMP(0);
    if (wave == 0 && lane < 32) *(LAS unsigned*)(lds + SYNC + lane * 4) = 0u;
    LDSBAR();
    {
        const u16* xt = xA + (size_t)rb * 64 * 512;
        const int kv0 = (hg >> 1) * 16;
#pragma unroll
        for (int i = 0; i < 6; ++i) { const int row = wave * 6 + i; issue_xrow(xt, row < kv0 ? row : row + 16, lds0 + RB, lane); }
        stage_w64<8>(wq, hg * 64, lds0, wave, lane);
        tagv = __builtin_amdgcn_raw_buffer_load_b32(rs, WS_EPOCH, 0, SC1);
        SBAR();
        DRAIN_VM();
        RAWBAR();
        STAMP(1);
        if (wave < 4) {
            const int hp = wave >> 1, ph = wave & 1;
            const int blk = (lane >> 4) & 1, q = (lane & 15) >> 2, p = lane & 3;
            const LAS char* pa = lds + (8 * h + q) * 128 + (((hp * 4 + blk * 2 + (p >> 1)) ^ (((q >> 1) & 1) << 2)) << 4) + (p & 1) * 8;
            const LAS char* xrow = lds + RB + (ph * 32 + r) * 1024;
            const LAS char* xp[8];
#pragma unroll
            for (int i = 0; i < 8; ++i) xp[i] = xrow + (((2 * i + h) ^ (r & 15)) << 4);
            f32x16 acc = {0};
            constexpr int PD = 4;
            bf16x8 xf[PD + 1], wf[PD + 1];
#pragma unroll
            for (int s = 0; s < PD; ++s) { xf[s] = *(const LAS bf16x8*)(xp[s & 7] + (s >> 3) * 256); wf[s] = trfrag(pa + s * 2048, pa + s * 2048 + 512); }
            SBAR();
#pragma unroll
            for (int s = 0; s < 32; ++s) {
                if (s + PD < 32) { xf[(s + PD) % (PD + 1)] = *(const LAS bf16x8*)(xp[(s + PD) & 7] + ((s + PD) >> 3) * 256); wf[(s + PD) % (PD + 1)] = trfrag(pa + (s + PD) * 2048, pa + (s + PD) * 2048 + 512); }
                SBAR();
                acc = MFMA32(wf[s % (PD + 1)], xf[s % (PD + 1)], acc);
                SBAR();
            }
            LAS char* dst = lds + QIMG + (hp * 128 + ph * 32 + r) * 32 + h * 16;
            *(LAS u32x4*)(dst) = (u32x4){cvtpk(acc[0] * QSCALE, acc[1] * QSCALE), cvtpk(acc[2] * QSCALE, acc[3] * QSCALE), cvtpk(acc[4] * QSCALE, acc[5] * QSCALE), cvtpk(acc[6] * QSCALE, acc[7] * QSCALE)};
            *(LAS u32x4*)(dst + 64 * 32) = (u32x4){cvtpk(acc[8] * QSCALE, acc[9] * QSCALE), cvtpk(acc[10] * QSCALE, acc[11] * QSCALE), cvtpk(acc[12] * QSCALE, acc[13] * QSCALE), cvtpk(acc[14] * QSCALE, acc[15] * QSCALE)};
        }
        STAMP(2);
        LDSBAR();
        STAMP(3);
    }
    const unsigned tag = rfl(tagv) + 1u;
    const unsigned sA = (unsigned)(2 * kh) * SLOT, sB = sA + SLOT;
    unsigned agen = 0;
    issue_chunk(kvg, 4 * kh, lds0 + sA, qp, lane);
    bf16x8 qfa = *(const LAS bf16x8*)(lds + QIMG + (qp * 64 + r) * 32 + h * 16);
    bf16x8 qfb = *(const LAS bf16x8*)(lds + QIMG + (qp * 64 + 32 + r) * 32 + h * 16);
    DRAIN_VM();
    RAWBAR();
    const int kr = h * 512 + r * 16, vr = 8192 + h * VBLK + (r < 17 ? r : 17) * 16;
    f32x16 oa, ob;
    float sha = 0.f, shb = 0.f;
    for (int attempt = 0;; ++attempt) {
        STAMP(4); CSTAMP(12);
        bf16x8 kc = *(const LAS bf16x8*)(lds + sA + kr);
        bf16x8 kn = *(const LAS bf16x8*)(lds + sA + kr + 1024);
        bf16x8 v0 = *(const LAS bf16x8*)(lds + sA + vr);
        bf16x8 v1 = *(const LAS bf16x8*)(lds + sA + vr + 2 * VBLK);
        f32x16 cia, cib, sc;
        if (attempt == 0) {
            const f32x16 z = {0};
            float ma = -INFINITY, mb = -INFINITY;
#pragma unroll
            for (int i = 0; i < NSAMP; ++i) {
                const bf16x8 k0 = *(const LAS bf16x8*)(lds + kr + i * 1024);
                ma = fmaxf(ma, tilemax(MFMA32(k0, qfa, z))); mb = fmaxf(mb, tilemax(MFMA32(k0, qfb, z)));
            }
            sha = swapmax(ma) + SHOFF;
            shb = swapmax(mb) + SHOFF;
        }
        {
            const f32x16 z = {0};
            unsigned msk = h == 0 ? 0xffffu : 0u, z0 = 0u;
            PINV(msk); PINV(z0);
            const bf16x8 e0 = as_frag((u32x4){msk & 0x3f80u, z0, z0, z0});
            cia = MFMA32(e0, as_frag((u32x4){cvtpk(-sha, 0.f) & msk, z0, z0, z0}), z);
            cib = MFMA32(e0, as_frag((u32x4){cvtpk(-shb, 0.f) & msk, z0, z0, z0}), z);
            oa = MFMA32(as_frag((u32x4){z0, z0, z0, z0}), e0, z);
            PINV(z0);
            ob = MFMA32(as_frag((u32x4){z0, z0, z0, z0}), e0, z);
        }
        sc = MFMA32(kc, qfa, cia);
        bf16x8 v1p = v1; u32x4 pp = {0u, 0u, 0u, 0u};
        unsigned s_cur = sA, s_nxt = sB;
#define EX(i) __builtin_amdgcn_exp2f(sc[i])
#define VTILE(PEND, NEXT, MINE, MID) do { \
            f32x16 sn; u32x4 p0, p1; \
            float e0 = EX(0), e1 = EX(1); PINV(e0); PINV(e1); SBAR(); \
            PEND; SBAR(); \
            float e2 = EX(2), e3 = EX(3); p0[0] = cvtpk(e0, e1); float e4 = EX(4), e5 = EX(5); p0[1] = cvtpk(e2, e3); float e6 = EX(6); \
            PINV(p0); PINV(e4); PINV(e5); PINV(e6); SBAR(); \
            sn = NEXT; PINV(sn); SBAR(); \
            float e7 = EX(7); p0[2] = cvtpk(e4, e5); float e8 = EX(8); p0[3] = cvtpk(e6, e7); PINV(p0); PINV(e8); SBAR(); \
            float e9 = EX(9), e10 = EX(10), e11 = EX(11); PINV(e9); PINV(e10); PINV(e11); SBAR(); \
            MINE; SBAR(); \
            MID; \
            float e12 = EX(12); p1[0] = cvtpk(e8, e9); float e13 = EX(13); p1[1] = cvtpk(e10, e11); float e14 = EX(14), e15 = EX(15); \
            p1[2] = cvtpk(e12, e13); p1[3] = cvtpk(e14, e15); PINV(p1); SBAR(); \
            pp = p1; sc = sn; } while (0)
        for (int j = 0; j < 4; ++j) {
            const LAS char* kcp = lds + s_cur + kr; const LAS char* vcp = lds + s_cur + vr;
            const LAS char* kxp = lds + s_nxt + kr; const LAS char* vxp = lds + s_nxt + vr;
#pragma unroll
            for (int t = 0; t < 8; ++t) {
                bf16x8 k2, nv0, nv1;
                if (t == 0) __builtin_amdgcn_s_setprio(2); else if (t == 3) __builtin_amdgcn_s_setprio(1); else if (t == 6) __builtin_amdgcn_s_setprio(0);
                VTILE({ ob = MFMA32(v1p, as_frag(pp), ob); PINV(ob); }, MFMA32(kc, qfb, cib), { oa = MFMA32(v0, as_frag(p0), oa); PINV(oa); },
                      { k2 = (t < 6) ? *(const LAS bf16x8*)(kcp + (t + 2) * 1024) : *(const LAS bf16x8*)(kxp + (t - 6) * 1024); SBAR();
                        nv1 = (t < 7) ? *(const LAS bf16x8*)(vcp + (t + 1) * 4 * VBLK + 2 * VBLK) : *(const LAS bf16x8*)(vxp + 2 * VBLK); SBAR();
                        nv0 = (t < 7) ? *(const LAS bf16x8*)(vcp + (t + 1) * 4 * VBLK) : *(const LAS bf16x8*)(vxp); SBAR(); });
                VTILE({ oa = MFMA32(v1, as_frag(pp), oa); PINV(oa); }, MFMA32(kn, qfa, cia), { ob = MFMA32(v0, as_frag(p0), ob); PINV(ob); }, { });
                v1p = v1; v1 = nv1; v0 = nv0; kc = kn; kn = k2;
                if (t == 0) {
                    if (j >= 1) sig<S_CONS>(syk);
                    else if (attempt == 0) { await<0, S_KVALL>(sy, 4); issue_chunk(kvg, 4 * kh + 1, lds0 + s_nxt, qp, lane); }
                    SBAR();
                }
                if (t == 1) {
                    if (j == 1 || j == 2) {
                        await<0, S_CONS>(syk, 12 * attempt + 4 * j);
                        issue_chunk(kvg, 4 * kh + j + 1, lds0 + s_nxt, qp, lane);
                    }
                    SBAR();
                }
                if (t == 4) { if (j < 3) { DRAIN_VM(); sig<S_LAND>(syk); } SBAR(); }
                if (t == 5) {
                    if (j < 3) await<0, S_LAND>(syk, 12 * attempt + 4 * (j + 1));
                    SBAR();
                }
            }
            const unsigned tmp = s_cur; s_cur = s_nxt; s_nxt = tmp;
        }
        ob = MFMA32(v1p, as_frag(pp), ob);
#undef EX
#undef VTILE
        __builtin_amdgcn_s_setprio(3);
        sig<S_AEND>(sy);
        STAMP(9);
        int lane2 = lane; PINV(lane2);
        const int r2 = lane2 & 31, h2 = lane2 >> 5;
        LAS char* part = lds + 2 * SLOT + qp * 4352;
        if (kh == 1) {
            await<0, S_CONS>(syk, 12 * attempt + 12);
            *(LAS f32x4*)(part + lane2 * 16) = (f32x4){oa[0], oa[1], oa[2], oa[3]};
            *(LAS f32x4*)(part + 1024 + lane2 * 16) = (f32x4){oa[4], oa[5], oa[6], oa[7]};
            *(LAS f32x4*)(part + 2176 + lane2 * 16) = (f32x4){ob[0], ob[1], ob[2], ob[3]};
            *(LAS f32x4*)(part + 2176 + 1024 + lane2 * 16) = (f32x4){ob[4], ob[5], ob[6], ob[7]};
            if (h2 == 0) { *(LAS float*)(part + 2048 + r2 * 4) = oa[8]; *(LAS float*)(part + 2176 + 2048 + r2 * 4) = ob[8]; }
            sig<S_DONE1>(sy + 4 * qp);
        } else {
            await<1, S_DONE1>(sy + 4 * qp, attempt + 1);
            const f32x4 a0 = *(const LAS f32x4*)(part + lane2 * 16), a1 = *(const LAS f32x4*)(part + 1024 + lane2 * 16);
            const f32x4 b0 = *(const LAS f32x4*)(part + 2176 + lane2 * 16), b1 = *(const LAS f32x4*)(part + 2176 + 1024 + lane2 * 16);
            const float la = oa[8] + *(const LAS float*)(part + 2048 + r2 * 4), lb = ob[8] + *(const LAS float*)(part + 2176 + 2048 + r2 * 4);
            const float sa = swapsum(h2 == 0 ? la : 0.f), sb_ = swapsum(h2 == 0 ? lb : 0.f);
            const float ra = __builtin_amdgcn_rcpf(sa), rb_ = __builtin_amdgcn_rcpf(sb_);
            float amax = 0.f;
#pragma unroll
            for (int i = 0; i < 4; ++i) {
                oa[i] += a0[i]; oa[4 + i] += a1[i]; ob[i] += b0[i]; ob[4 + i] += b1[i];
            }
            const unsigned off = WS_OA + (unsigned)((rb * 32 + hg * 4 + qp) * 64 + r2) * 32u + h2 * 16u;
            __builtin_amdgcn_raw_buffer_store_b128((u32x4){cvtpk(oa[0] * ra, oa[1] * ra), cvtpk(oa[2] * ra, oa[3] * ra), cvtpk(oa[4] * ra, oa[5] * ra), cvtpk(oa[6] * ra, oa[7] * ra)}, rs, off, 0, SC1);
            __builtin_amdgcn_raw_buffer_store_b128((u32x4){cvtpk(ob[0] * rb_, ob[1] * rb_), cvtpk(ob[2] * rb_, ob[3] * rb_), cvtpk(ob[4] * rb_, ob[5] * rb_), cvtpk(ob[6] * rb_, ob[7] * rb_)}, rs, off + 1024u, 0, SC1);
            SBAR();
            bool ok = (sa >= LMIN) && (sa <= 1.2676506e30f) && (sb_ >= LMIN) && (sb_ <= 1.2676506e30f);
#pragma unroll
            for (int i = 0; i < 8; ++i) ok = ok && (fabsf(oa[i]) <= 3.0e38f) && (fabsf(ob[i]) <= 3.0e38f);
            (void)amax;
            const bool bad = attempt == 0 && !__all(ok);
            (void)sig_rtn<S_VERD>(sy, bad ? 0x10001u : 1u);
            DRAIN_VM();
            const unsigned tot = sig_rtn<S_PUB>(sy, 1u) + 1u;
            if (tot == 4u * (attempt + 1)) {
                unsigned vd;
                for (unsigned spins = 0; ((vd = peek<S_VERD>(sy)) & 0xffffu) < 4u * (attempt + 1) && spins < (1u << 18); ++spins) { }
                if (attempt == 1 || (vd >> 16) == 0) {
                    unsigned tg = tag; PINV(tg);
                    if (lane2 == 0) __builtin_amdgcn_raw_buffer_store_b32(tg, rs, WS_FLAG_A + (rb * 8 + hg) * FSTR, 0, SC1);
                }
            }
        }
        STAMP(5); CSTAMP(13);
        unsigned pub;
        for (unsigned spins = 0; ((pub = peek<S_VERD>(sy)) & 0xffffu) < 4u * (attempt + 1) && spins < (1u << 18); ++spins) __builtin_amdgcn_s_sleep(1);
        if (attempt == 1 || (pub >> 16) == 0) break;
        float ma = -INFINITY, mb = -INFINITY;
        for (int t = 0; t < 64; ++t) {
            const u32x4 kf = __builtin_amdgcn_raw_buffer_load_b128(rs, WS_KV + (t >> 3) * SLOT + (t & 7) * 1024 + kr, 0, SC1);
            const f32x16 z = {0};
            ma = fmaxf(ma, tilemax(MFMA32(as_frag(kf), qfa, z)));
            mb = fmaxf(mb, tilemax(MFMA32(as_frag(kf), qfb, z)));
        }
        sha = swapmax(ma); shb = swapmax(mb);
        gbar<S_ACNT>(sy, agen, 8);
        issue_chunk(kvg, 4 * kh, lds0 + sA, qp, lane); issue_chunk(kvg, 4 * kh + 1, lds0 + sB, qp, lane);
        DRAIN_VM();
        gbar<S_ACNT>(sy, agen, 8);
    }
    tag_out = tag;
    if (kh == 0) STAMPDUMP();
    if (b == 0 && wave == 0) {
        const unsigned long long t0 = __builtin_amdgcn_s_memrealtime();
        for (;;) {
            unsigned v[8];
#pragma unroll
            for (int i = 0; i < 8; ++i) v[i] = __builtin_amdgcn_raw_buffer_load_b32(rs, WS_FLAG_A + (i * 64 + lane) * FSTR, 0, SC1);
            bool all = true;
#pragma unroll
            for (int i = 0; i < 8; ++i) all = all && v[i] == tag;
            if (__all(all) || timed_out(rs, t0)) break;
            __builtin_amdgcn_s_sleep(2);
        }
        if (lane == 0) __builtin_amdgcn_raw_buffer_store_b32(tag, rs, WS_EPOCH, 0, SC1);
    }
}

DI void role_b(const u16* xA, const u16* wq, const u16* xB, const u16* wqkv, const u16* wk, const u16* wv, const u16* woA, const u16* woB, u16* out,
               char* ws, rsrc_t rs, LAS char* ldsall, int b, int rb, int hg, int wave, unsigned& tag_out, unsigned& gen_out) {
    const int lane = threadIdx.x & 63;
    const int r = lane & 31, h = lane >> 5;
    const int g = lane >> 4, c16 = lane & 15;
    LAS char* lds = ldsall + RB;
    const unsigned lds0 = (unsigned)(uintptr_t)lds;
    const unsigned sy = (unsigned)(uintptr_t)ldsall + SYNC;
    unsigned tag = 0, tagv, gen = 0;
    __builtin_amdgcn_s_setprio(BPRIO);
    STAMP(0);
    const int wch = (lane & 7) ^ (((lane >> 4) & 1) << 2);
    const unsigned av0 = (unsigned)(((wave & 1) * 32 + (lane >> 3)) * 3072 + (wave >> 1) * 1024 + hg * 128 + wch * 16);
    unsigned bv[4], cv;
    {
        const int che = (lane & 7) ^ ((lane >> 4) & 7), cho = (lane & 7) ^ ((4 + (lane >> 4)) & 7);
#pragma unroll
        for (int i = 0; i < 4; ++i) {
            int gr = rb * 64 - 16 + ((wave & 1) * 4 + i) * 8 + (lane >> 3); gr = gr < 0 ? 0 : gr;
            const unsigned xo = (unsigned)(gr * 1024 + ((i & 1) ? cho : che) * 16 - i * 1024 + 4096);
            const unsigned wo = (unsigned)(((wave & 1) * 32 + i * 8 + (lane >> 3)) * 3072 + 2048 + hg * 128 + wch * 16 - i * 1024);
            bv[i] = wave < 2 ? wo : xo;
        }
        cv = wave < 2 ? (unsigned)((rb * 64 - 16 + (8 + wave) * 8 + (lane >> 3)) * 1024 + (wave ? cho : che) * 16 + 4096) : bv[0];
    }
    const char* srcA = (const char*)wqkv;
    const char* srcB = wave < 2 ? (const char*)wqkv : (const char*)xB - 4096;
    const char* srcC = (const char*)xB - 4096;
    const unsigned advB = wave < 2 ? 64 * 3072 : 128;
    const unsigned dA = B1_X + wave * 4096, dB = wave < 2 ? B1_X + 16384 + wave * 4096 : (wave - 2) * 4096, dC = wave < 2 ? (8 + wave) * 1024 : dB;
    int pfr = rb * 64 - 16 + lane; pfr = pfr < 0 ? 0 : pfr;
    const unsigned pfo = wave < 3 ? (unsigned)(lane * 3072 + wave * 1024 + hg * 128) : (unsigned)(pfr * 1024);
    const unsigned pfo2 = (unsigned)((rb * 64 + 48 + (lane & 15)) * 1024);
    auto touch_step = [&](int k) {
    };
    auto issue_step = [&](unsigned buf) {
        dmag4(av0, av0 + 23552, av0 + 47104, av0 + 70656, srcA, rfl(lds0 + buf + dA));
        dmag4(bv[0], bv[1], bv[2], bv[3], srcB, rfl(lds0 + buf + dB));
        dmag1(cv, srcC, rfl(lds0 + buf + dC));
        srcA += 64 * 3072; srcB += advB; srcC += 128;
    };
    {
        const int which = hg & 1, kv0 = (hg >> 1) * 16, t16 = rb * 4 + (hg >> 1);
        const u16* xt = xA + (size_t)rb * 64 * 512;
        {
            const u16* W = which ? wv : wk;
            dma4(lane * 16, (const char*)W + wave * 4096, rfl(lds0 + XCH + wave * 4096));
#pragma unroll
            for (int i = 0; i < 4; ++i) issue_xrow(xt, kv0 + wave * 4 + i, lds0, lane);
            tagv = __builtin_amdgcn_raw_buffer_load_b32(rs, WS_EPOCH, 0, SC1);
            SBAR();
        }
        RAWBAR();
        DRAIN_VM();
        gbar<S_BCNT>(sy, gen, 4);
        tag = rfl(tagv) + 1u;
        f32x4 acc;
        {
            STAMP(7);
            const LAS char* base = lds + XCH + (8 * g + (c16 >> 2)) * 32 + (lane & 3) * 8 + wave * 4096;
            const LAS char* xrow = lds + (kv0 + c16) * 1024 + wave * 256;
            bf16x8 xf[4], wf[4];
#pragma unroll
            for (int s = 0; s < 4; ++s) { xf[s] = *(const LAS bf16x8*)(xrow + (((4 * s + g) ^ c16) << 4)); wf[s] = trfrag(base + s * 1024, base + s * 1024 + 128); }
            f32x4 a1 = {0, 0, 0, 0}, a2 = {0, 0, 0, 0};
            if (which == 0) { a1 = MFMA16(wf[0], xf[0], a1); a2 = MFMA16(wf[1], xf[1], a2); a1 = MFMA16(wf[2], xf[2], a1); a2 = MFMA16(wf[3], xf[3], a2); }
            else { a1 = MFMA16(xf[0], wf[0], a1); a2 = MFMA16(xf[1], wf[1], a2); a1 = MFMA16(xf[2], wf[2], a1); a2 = MFMA16(xf[3], wf[3], a2); }
            acc = a1 + a2;
            if (wave != 0) *(LAS f32x4*)(ldsall + QIMG + wave * 1024 + lane * 16) = acc;
            gbar<S_BCNT>(sy, gen, 4);
        }
        if (wave == 0) {
#pragma unroll
            for (int i = 1; i < 4; ++i) acc += *(const LAS f32x4*)(ldsall + QIMG + i * 1024 + lane * 16);
            const u32x2 w = {cvtpk(acc[0], acc[1]), cvtpk(acc[2], acc[3])};
            const int t32 = t16 >> 1, s16 = t16 & 1, t = t32 & 7;
            const unsigned cbase = WS_KV + (t32 >> 3) * SLOT;
            const unsigned vb = cbase + 8192 + (t * 2 + s16) * 2 * VBLK;
            const unsigned off = which ? vb + (g & 1) * VBLK + c16 * 16 + (g >> 1) * 8 : cbase + t * 1024 + (g & 1) * 512 + (s16 * 16 + c16) * 16 + (g >> 1) * 8;
            __builtin_amdgcn_raw_buffer_store_b64(w, rs, off, 0, SC1);
            if (which && lane < 8) {
                const unsigned val = (lane & 2) ? 0u : 0x3f803f80u;
                __builtin_amdgcn_raw_buffer_store_b64((u32x2){val, val}, rs, vb + (lane >> 2) * VBLK + 256 + (lane & 3) * 8, 0, SC1);
            }
            DRAIN_VM();
            if (lane == 0) __builtin_amdgcn_raw_buffer_store_b32(tag, rs, WS_FLAG_KV + (rb * 8 + hg) * FSTR, 0, SC1);
            STAMP(8);
        }
        RAWBAR();
        if (wave == 0) poll_flags(rs, WS_FLAG_KV + ((lane & 31) + (lane >> 5) * 128) * FSTR, true, tag);
        STAMP(9);
        RAWBAR();
        RAWBAR();
        if (wave == 1 && lane < 16) *(LAS u32x4*)(lds + (lane >> 3) * B1_ST + 10240 + (lane & 7) * 16) = (u32x4){0u, 0u, 0u, 0u};
        poll_flags(rs, WS_FLAG_KV + (wave * 64 + lane) * FSTR, true, tag);
        sig<S_KVALL>(sy);
        issue_step(0);
    }
    {
        const int rtA = (wave == 0) ? 0 : (wave == 3 ? 2 : 1), rtB = (wave == 0) ? 0 : (wave == 1 ? 1 : 2);
        const int sgA = (wave == 0) ? 1 : (wave == 2 ? 2 : 0), sgB = (wave == 0) ? 2 : (wave == 2 ? 2 : 1);
        int irA = rtA * 32 - 16 + r; irA = irA < 0 ? ZROW : irA;
        int irB = rtB * 32 - 16 + r; irB = irB < 0 ? ZROW : irB;
        int xa[4], xb_[4];
#pragma unroll
        for (int i = 0; i < 4; ++i) { xa[i] = irA * 128 + (((2 * i + h) ^ ((irA >> 1) & 7)) << 4); xb_[i] = irB * 128 + (((2 * i + h) ^ ((irB >> 1) & 7)) << 4); }
        const int blk = (lane >> 4) & 1, q = (lane & 15) >> 2, p = lane & 3;
        const int ch0 = (blk * 2 + (p >> 1)) ^ (((q >> 1) & 1) << 2);
        const int rdl = B1_X + (8 * h + q) * 128 + (p & 1) * 8;
        const int a0 = rdl + sgA * 8192 + ch0 * 16, a1 = rdl + sgA * 8192 + (ch0 ^ 4) * 16;
        const int b0 = rdl + sgB * 8192 + ch0 * 16, b1 = rdl + sgB * 8192 + (ch0 ^ 4) * 16;
        f32x16 acc0 = {0}, acc1 = {0}, acc2 = {0}, acc3 = {0};
        unsigned buf = 0;
#pragma unroll 1
        for (int k = 0; k < 8; ++k) {
            DRAIN_VM();
            gbar<S_BCNT, BSLP>(sy, gen, 4);
            const bool paced = true;
            if (k < 7) issue_step(B1_ST - buf);
            if (k < 6) touch_step(k + 2);
            if (k == 5) {
                l2_touch((unsigned)((wave * 128 + lane) * 1024 + hg * 128), woB, sy + 640); l2_touch((unsigned)((wave * 128 + 64 + lane) * 1024 + hg * 128), woB, sy + 640);
            }
            const LAS char* sb = lds + buf;
#pragma unroll
            for (int hs = 0; hs < 2; ++hs) {
                bf16x8 fx[2][2], fw[2][4];
#pragma unroll
                for (int s = 0; s < 2; ++s) {
                    const int ks = 2 * hs + s;
                    fx[s][0] = *(const LAS bf16x8*)(sb + xa[ks]);
                    fw[s][0] = trfrag(sb + a0 + ks * 2048, sb + a0 + ks * 2048 + 512); fw[s][1] = trfrag(sb + a1 + ks * 2048, sb + a1 + ks * 2048 + 512);
                    fx[s][1] = *(const LAS bf16x8*)(sb + xb_[ks]);
                    fw[s][2] = trfrag(sb + b0 + ks * 2048, sb + b0 + ks * 2048 + 512); fw[s][3] = trfrag(sb + b1 + ks * 2048, sb + b1 + ks * 2048 + 512);
                }
                SBAR();
#define BSTEP(ACC, FW, FX) do { ACC = MFMA32(FW, FX, ACC); PINV(ACC); SBAR(); if (paced) __builtin_amdgcn_s_sleep(PACE); SBAR(); } while (0)
                BSTEP(acc0, fw[0][0], fx[0][0]); BSTEP(acc1, fw[0][1], fx[0][0]); BSTEP(acc2, fw[0][2], fx[0][1]); BSTEP(acc3, fw[0][3], fx[0][1]);
                BSTEP(acc0, fw[1][0], fx[1][0]); BSTEP(acc1, fw[1][1], fx[1][0]); BSTEP(acc2, fw[1][2], fx[1][1]); BSTEP(acc3, fw[1][3], fx[1][1]);
#undef BSTEP
            }
            buf = B1_ST - buf;
        }
        gbar<S_BCNT>(sy, gen, 4);
        STAMP(2);
#define PK8(A, o) (u32x4){cvtpk(A[o], A[o + 1]), cvtpk(A[o + 2], A[o + 3]), cvtpk(A[o + 4], A[o + 5]), cvtpk(A[o + 6], A[o + 7])}
        if (rtA != 0 || r >= 16) {
            LAS char* qa = lds + (rtA * 32 - 16 + r) * QPITCH + sgA * 128 + h * 16;
            *(LAS u32x4*)(qa + 0) = PK8(acc0, 0); *(LAS u32x4*)(qa + 32) = PK8(acc0, 8); *(LAS u32x4*)(qa + 64) = PK8(acc1, 0); *(LAS u32x4*)(qa + 96) = PK8(acc1, 8);
            LAS char* qb = lds + (rtB * 32 - 16 + r) * QPITCH + sgB * 128 + h * 16;
            *(LAS u32x4*)(qb + 0) = PK8(acc2, 0); *(LAS u32x4*)(qb + 32) = PK8(acc2, 8); *(LAS u32x4*)(qb + 64) = PK8(acc3, 0); *(LAS u32x4*)(qb + 96) = PK8(acc3, 8);
        }
#undef PK8
        gbar<S_BCNT>(sy, gen, 4);
    }
#pragma unroll 1
    for (int ph = 0; ph < 2; ++ph) {
        const int pos = ph * 32 + r;
        const LAS char* row = lds + (16 + pos) * QPITCH + wave * 32;
        const u32x4 q0 = *(const LAS u32x4*)(row), q1 = *(const LAS u32x4*)(row + 16);
        const LAS char* krow = row + 128 - 8 * h * QPITCH;
        float sc[4];
#pragma unroll
        for (int i = 0; i < 4; ++i) {
            const u32x4 k0 = *(const LAS u32x4*)(krow - 2 * i * QPITCH), k1 = *(const LAS u32x4*)(krow - 2 * i * QPITCH + 16);
            float s = 0.f, s2 = 0.f;
#pragma unroll
            for (int j = 0; j < 4; ++j) { s = dot2bf(q0[j], k0[j], s); s2 = dot2bf(q1[j], k1[j], s2); }
            sc[i] = (rb * 64 + pos - 2 * (4 * h + i) >= 0) ? (s + s2) * QSCALE : -INFINITY;
        }
        const float m = swapmax(fmaxf(fmaxf(sc[0], sc[1]), fmaxf(sc[2], sc[3])));
        float pw[4];
#pragma unroll
        for (int i = 0; i < 4; ++i) pw[i] = __builtin_amdgcn_exp2f(sc[i] - m);
        const float l = swapsum((pw[0] + pw[1]) + (pw[2] + pw[3]));
        const unsigned pa = cvtpk(pw[0], pw[1]), pb = cvtpk(pw[2], pw[3]);
        const auto ra = __builtin_amdgcn_permlane32_swap(pa, pa, false, false);
        const auto rb2 = __builtin_amdgcn_permlane32_swap(pb, pb, false, false);
        const unsigned pp[4] = {ra[0], rb2[0], ra[1], rb2[1]};
        float o[8];
#pragma unroll
        for (int j = 0; j < 8; ++j) o[j] = 0.f;
        const LAS char* vrow = row + 256 + h * 16;
#pragma unroll
        for (int w2 = 0; w2 < 4; ++w2) {
            const u32x4 va = *(const LAS u32x4*)(vrow - 4 * w2 * QPITCH), vb = *(const LAS u32x4*)(vrow - (4 * w2 + 2) * QPITCH);
#pragma unroll
            for (int j = 0; j < 4; ++j) {
                o[2 * j] = dot2bf(__builtin_amdgcn_perm(vb[j], va[j], 0x05040100u), pp[w2], o[2 * j]);
                o[2 * j + 1] = dot2bf(__builtin_amdgcn_perm(vb[j], va[j], 0x07060302u), pp[w2], o[2 * j + 1]);
            }
        }
        const float rl = __builtin_amdgcn_rcpf(l);
        u32x4 ow = {cvtpk(o[0] * rl, o[1] * rl), cvtpk(o[2] * rl, o[3] * rl), cvtpk(o[4] * rl, o[5] * rl), cvtpk(o[6] * rl, o[7] * rl)};
        __builtin_amdgcn_raw_buffer_store_b128(ow, rs, WS_OB + (unsigned)((rb * 32 + hg * 4 + wave) * 64 + pos) * 32u + h * 16u, 0, SC1);
    }
    DRAIN_VM();
    gbar<S_BCNT>(sy, gen, 4);
    if (wave == 0 && lane == 0) __builtin_amdgcn_raw_buffer_store_b32(tag, rs, WS_FLAG_B + (rb * 8 + hg) * FSTR, 0, SC1);
    STAMP(3);
    stage_w64<4>(woB, hg * 64, lds0, wave, lane);
#pragma unroll
    for (int i = 0; i < 2; ++i)
        asm volatile("s_mov_b32 m0, %2\n\ts_nop 0\n\tglobal_load_lds_dword %0, %1" :: "v"((unsigned)(((wave * 2 + i) * 64 + lane) * 1024 + hg * 128)), "s"(woA), "s"(sy + 640) : "memory");
    DRAIN_VM();
    gbar<S_BCNT, 1>(sy, gen, 4);
    if (wave == 0) { poll_flags(rs, WS_FLAG_B + (rb * 8 + (lane & 7)) * FSTR, lane < 8, tag); sig<S_OBRDY>(sy); }
    STAMP(4);
    {
        unsigned pub;
        for (unsigned spins = 0; spins < (1u << 18); ++spins) {
            pub = peek<S_VERD>(sy);
            if ((pub & 0xffffu) >= 8u || ((pub & 0xffffu) == 4u && (pub >> 16) == 0)) break;
            __builtin_amdgcn_s_sleep(2);
        }
    }
    stage_w64<4>(woA, hg * 64, (unsigned)(uintptr_t)ldsall, wave, lane);
    DRAIN_VM();
    gbar<S_BCNT>(sy, gen, 4);
    STAMP(5);
    tag_out = tag; gen_out = gen;
}

constexpr int OPD = 6;
DI void out_tile(int br, int wave, unsigned tag, unsigned gen, u16* out, char* ws, rsrc_t rs, LAS char* ldsall, int rb, int hg) {
    const int lane = threadIdx.x & 63;
    const int r = lane & 31, h = lane >> 5;
    const unsigned sy = (unsigned)(uintptr_t)ldsall + SYNC;
    const int kh = wave & 1, ph = wave >> 1;
    const int blk = (lane >> 4) & 1, q = (lane & 15) >> 2, p = lane & 3;
    const int ch0 = (blk * 2 + (p >> 1)) ^ (((q >> 1) & 1) << 2);
    const LAS char* rowp = ldsall + (br ? RB : 0) + (kh * 256 + 4 * h + q) * 128 + (p & 1) * 8;
    const LAS char* b0 = rowp + ch0 * 16; const LAS char* b1 = rowp + (ch0 ^ 4) * 16;
    if (br) await<1, S_OBRDY>(sy, 1);
    bf16x8 w0[16], w1[16];
#pragma unroll
    for (int s = 0; s < OPD; ++s) { w0[s] = trfrag(b0 + s * 2048, b0 + s * 2048 + 1024); w1[s] = trfrag(b1 + s * 2048, b1 + s * 2048 + 1024); }
#pragma unroll
    for (int s = 0; s < OPD; ++s) { PINV(w0[s]); PINV(w1[s]); }
    if (!br) poll_flags(rs, WS_FLAG_A + (rb * 8 + (lane & 7)) * FSTR, lane < 8, tag);
    STAMP(6);
    u32x4 bfr[16];
    const unsigned obase = (br ? WS_OB : WS_OA) + (unsigned)((rb * 32 + kh * 16) * 64 + ph * 32 + r) * 32u + h * 16u;
#pragma unroll
    for (int s = 0; s < 16; ++s) bfr[s] = __builtin_amdgcn_raw_buffer_load_b128(rs, obase + s * 2048u, 0, SC1);
    SBAR();
    f32x16 acc0 = {0}, acc1 = {0};
#pragma unroll
    for (int s = 0; s < 16; ++s) {
        acc0 = MFMA32(w0[s], as_frag(bfr[s]), acc0);
        acc1 = MFMA32(w1[s], as_frag(bfr[s]), acc1);
        if (s + OPD < 16) { w0[s + OPD] = trfrag(b0 + (s + OPD) * 2048, b0 + (s + OPD) * 2048 + 1024); w1[s + OPD] = trfrag(b1 + (s + OPD) * 2048, b1 + (s + OPD) * 2048 + 1024); }
        SBAR();
    }
    LAS f32x4* mine = (LAS f32x4*)(ldsall + RB + XCH + wave * 4096);
    const LAS f32x4* theirs = (const LAS f32x4*)(ldsall + RB + XCH + (wave ^ 1) * 4096);
    const f32x16 give = kh ? acc0 : acc1, keep = kh ? acc1 : acc0;
    if (!br) await<0, S_XB>(sy, 4);
#pragma unroll
    for (int i = 0; i < 4; ++i) mine[i * 64 + lane] = (f32x4){give[4 * i], give[4 * i + 1], give[4 * i + 2], give[4 * i + 3]};
    gbar<0>(sy + (br ? S_GB2 : S_BCNT), gen, 4);
    f32x4 t[4];
#pragma unroll
    for (int i = 0; i < 4; ++i) t[i] = theirs[i * 64 + lane];
    if (br) { asm volatile("s_waitcnt lgkmcnt(0)" ::: "memory"); sig<S_XB>(sy); }
    unsigned w[8];
#pragma unroll
    for (int i = 0; i < 4; ++i) { w[2 * i] = cvtpk(keep[4 * i] + t[i][0], keep[4 * i + 1] + t[i][1]); w[2 * i + 1] = cvtpk(keep[4 * i + 2] + t[i][2], keep[4 * i + 3] + t[i][3]); }
    u16* dst = out + (size_t)(rb * 64 + ph * 32 + r) * 1024 + br * 512 + hg * 64 + kh * 32 + h * 8;
#pragma unroll
    for (int i = 0; i < 2; ++i) {
        auto r0 = __builtin_amdgcn_permlane32_swap(w[4 * i], w[4 * i + 2], false, false);
        auto r1 = __builtin_amdgcn_permlane32_swap(w[4 * i + 1], w[4 * i + 3], false, false);
        *(GAS u32x4*)(dst + i * 16) = (u32x4){r0[0], r1[0], r0[1], r1[1]};
    }
    STAMP(10);
    STAMPDUMP();
}

__global__ void __launch_bounds__(768, 3) fused_kernel(const u16* xA, const u16* wq, const u16* wk, const u16* wv, const u16* woA,
                                                       const u16* xB, const u16* wqkv, const u16* woB, u16* out, char* ws) {
    extern __shared__ __attribute__((aligned(16))) char smem[];
    LAS char* lds = (LAS char*)smem;
    const rsrc_t rs = __builtin_amdgcn_make_buffer_rsrc(ws, 0, WS_TOTAL, 0x00020000);
    const int b = blockIdx.x;
    const int rb = ((b & 7) >> 1) * 8 + ((b >> 3) & 7), hg = (b & 1) * 4 + (b >> 6);
    const int wave = __builtin_amdgcn_readfirstlane(threadIdx.x >> 6);
    unsigned tag = 0, gen = 0;
    if (wave < 8) role_a(xA, wq, xB, wqkv, ws, rs, lds, b, rb, hg, wave, tag);
    else role_b(xA, wq, xB, wqkv, wk, wv, woA, woB, out, ws, rs, lds, b, rb, hg, wave - 8, tag, gen);
    if (wave >= 4) out_tile(wave < 8 ? 1 : 0, wave & 3, tag, gen, out, ws, rs, lds, rb, hg);
}

extern "C" void kernel_launch(void* const* d_in, const int* in_sizes, int n_in, void* d_out, int out_size, void* d_ws, size_t ws_size,
                              hipStream_t stream) {
    (void)in_sizes; (void)n_in; (void)out_size; (void)ws_size;
    (void)hipFuncSetAttribute((const void*)fused_kernel, hipFuncAttributeMaxDynamicSharedMemorySize, LDS_BYTES);
    fused_kernel<<<256, 768, LDS_BYTES, stream>>>((const u16*)d_in[0], (const u16*)d_in[1], (const u16*)d_in[2], (const u16*)d_in[3], (const u16*)d_in[4],
                                                  (const u16*)d_in[5], (const u16*)d_in[6], (const u16*)d_in[7], (u16*)d_out, (char*)d_ws);
}
```
